```python
import torch

def _softmax(x, axis=-1):
    return torch.softmax(x, dim=axis)
def _gelu(x):
    return torch.nn.functional.gelu(x, approximate="tanh")
def _silu(x):
    return torch.nn.functional.silu(x)
def _sigmoid(x):
    return torch.sigmoid(x)
def _layernorm(x, axis=-1, eps=1e-5):
    m = x.mean(dim=axis, keepdim=True)
    v = x.var(dim=axis, keepdim=True, unbiased=False)
    return (x - m) / torch.sqrt(v + eps)
def _scatter_add(tgt, idx, src, axis=0):
    return tgt.clone().index_add_(axis, idx, src)
def _segment_sum(src, seg, n):
    out = torch.zeros((n,) + src.shape[1:], dtype=src.dtype)
    return out.index_add_(0, seg, src)
def _pad(x, pw):
    flat = tuple(v for pair in reversed(pw) for v in pair)
    return torch.nn.functional.pad(x, flat)

kernel_name = 'concat_heads__gqa_block_cached__gqa_block_cached__near_ridge'

def setup_inputs():

    _w = (lambda a: (lambda t: t.to(torch.bfloat16) if t.is_floating_point() else t)(torch.as_tensor(a)))
    def _A_setup():
        B, Sq, D = 8, 208, 640
        Hq, Hkv, Sk, Dh = 32, 32, 1024, 128
        assert Hq % Hkv == 0, (Hq, Hkv)
        return {
            "x": _w((lambda *s: torch.randn(*s, dtype=torch.bfloat16) * 0.8)(B, Sq, D)),
            "k_cache": _w((lambda *s: torch.randn(*s, dtype=torch.bfloat16) * 0.8)(B, Hkv, Sk, Dh)),
            "v_cache": _w((lambda *s: torch.randn(*s, dtype=torch.bfloat16) * 0.05)(B, Hkv, Sk, Dh)),
            "wq": _w((lambda *s: torch.randn(*s, dtype=torch.bfloat16) * 0.8)(D, Hq * Dh) * (D ** -0.5)),
            "wo": _w((lambda *s: torch.randn(*s, dtype=torch.bfloat16) * 0.05)(Hq * Dh, D) * ((Hq * Dh) ** -0.5)),
        }
    _A_in = _A_setup()

    def _B_setup():
        B, Sq, D = 8, 208, 640
        Hq, Hkv, Sk, Dh = 32, 32, 1024, 128
        assert Hq % Hkv == 0, (Hq, Hkv)
        return {
            "x": _w((lambda *s: torch.randn(*s, dtype=torch.bfloat16) * 0.8)(B, Sq, D)),
            "k_cache": _w((lambda *s: torch.randn(*s, dtype=torch.bfloat16) * 0.8)(B, Hkv, Sk, Dh)),
            "v_cache": _w((lambda *s: torch.randn(*s, dtype=torch.bfloat16) * 0.05)(B, Hkv, Sk, Dh)),
            "wq": _w((lambda *s: torch.randn(*s, dtype=torch.bfloat16) * 0.8)(D, Hq * Dh) * (D ** -0.5)),
            "wo": _w((lambda *s: torch.randn(*s, dtype=torch.bfloat16) * 0.05)(Hq * Dh, D) * ((Hq * Dh) ** -0.5)),
        }
    _B_in = _B_setup()

    return {
        "A_x": _A_in["x"],
        "A_k_cache": _A_in["k_cache"],
        "A_v_cache": _A_in["v_cache"],
        "A_wq": _A_in["wq"],
        "A_wo": _A_in["wo"],
        "B_x": _B_in["x"],
        "B_k_cache": _B_in["k_cache"],
        "B_v_cache": _B_in["v_cache"],
        "B_wq": _B_in["wq"],
        "B_wo": _B_in["wo"],
    }

def reference(A_x, A_k_cache, A_v_cache, A_wq, A_wo, B_x, B_k_cache, B_v_cache, B_wq, B_wo):
    def _A_ref(x, k_cache, v_cache, wq, wo):
        B, Sq, D = x.shape
        Hq, Dh = 32, 128
        Hkv = k_cache.shape[1]
        rep = Hq // Hkv
        q = (lambda x, *d: x.permute(*d))((x @ wq).reshape(B, Sq, Hq, Dh), 0, 2, 1, 3)
        k = (lambda x, n, axis: torch.repeat_interleave(x, n, dim=axis))(k_cache, rep, axis=1)
        v = (lambda x, n, axis: torch.repeat_interleave(x, n, dim=axis))(v_cache, rep, axis=1)
        scores = torch.einsum("bhqd,bhkd->bhqk", q, k) * (Dh ** -0.5)
        p = _softmax(scores, axis=-1)
        o = torch.einsum("bhqk,bhkd->bhqd", p, v)
        return (lambda x, *d: x.permute(*d))(o, 0, 2, 1, 3).reshape(B, Sq, Hq * Dh) @ wo

    def _B_ref(x, k_cache, v_cache, wq, wo):
        B, Sq, D = x.shape
        Hq, Dh = 32, 128
        Hkv = k_cache.shape[1]
        rep = Hq // Hkv
        q = (lambda x, *d: x.permute(*d))((x @ wq).reshape(B, Sq, Hq, Dh), 0, 2, 1, 3)
        k = (lambda x, n, axis: torch.repeat_interleave(x, n, dim=axis))(k_cache, rep, axis=1)
        v = (lambda x, n, axis: torch.repeat_interleave(x, n, dim=axis))(v_cache, rep, axis=1)
        scores = torch.einsum("bhqd,bhkd->bhqk", q, k) * (Dh ** -0.5)
        p = _softmax(scores, axis=-1)
        o = torch.einsum("bhqk,bhkd->bhqd", p, v)
        return (lambda x, *d: x.permute(*d))(o, 0, 2, 1, 3).reshape(B, Sq, Hq * Dh) @ wo

    a = _A_ref(A_x, A_k_cache, A_v_cache, A_wq, A_wo)
    b = _B_ref(B_x, B_k_cache, B_v_cache, B_wq, B_wo)
    return torch.concatenate([a, b], axis=-1)
```

```cpp
#include <hip/hip_runtime.h>
#include <hip/hip_bf16.h>
#include <cstdio>

using bf16 = __hip_bfloat16;
using bf16x8 = __attribute__((ext_vector_type(8))) short;
using s16x4  = __attribute__((ext_vector_type(4))) short;
using f32x16 = __attribute__((ext_vector_type(16))) float;
using f32x4  = __attribute__((ext_vector_type(4))) float;
using u32x4  = __attribute__((ext_vector_type(4))) unsigned;

constexpr int NB = 8, SQ = 208, DM = 640, H = 32, SK = 1024, DH = 128, M = NB * SQ, HD = H * DH;

__device__ __forceinline__ unsigned short f2bf(float x) { __bf16 b = (__bf16)x; return __builtin_bit_cast(unsigned short, b); }

__global__ __launch_bounds__(256) void gemm_simple(const unsigned short* __restrict__ A0, const unsigned short* __restrict__ B0, unsigned short* __restrict__ C0,
                            const unsigned short* __restrict__ A1, const unsigned short* __restrict__ B1, unsigned short* __restrict__ C1,
                            int Mm, int N, int K, int lda, int ldb, int ldc, float scale) {
  const unsigned short* A = blockIdx.z ? A1 : A0; const unsigned short* Bm = blockIdx.z ? B1 : B0; unsigned short* C = blockIdx.z ? C1 : C0;
  __shared__ __attribute__((aligned(16))) unsigned short Bs[32][64 + 8];
  int wid = threadIdx.x >> 6, lane = threadIdx.x & 63, r = lane & 31, h = lane >> 5;
  int m0 = blockIdx.y * 128 + wid * 32, n0 = blockIdx.x * 32;
  f32x16 acc = {};
  for (int k0 = 0; k0 < K; k0 += 64) {
    __syncthreads();
    for (int i = threadIdx.x; i < 64 * 32; i += 256) { int kk = i >> 5, nn = i & 31; Bs[nn][kk] = Bm[(long)(k0 + kk) * ldb + n0 + nn]; }
    __syncthreads();
    for (int s = 0; s < 4; ++s) {
      bf16x8 a = *(const bf16x8*)(A + (long)(m0 + r) * lda + k0 + 16 * s + 8 * h);
      bf16x8 b = *(const bf16x8*)(&Bs[r][16 * s + 8 * h]);
      acc = __builtin_amdgcn_mfma_f32_32x32x16_bf16(a, b, acc, 0, 0, 0);
    }
  }
  for (int i = 0; i < 16; ++i) { int row = (i & 3) + 8 * (i >> 2) + 4 * h; C[(long)(m0 + row) * ldc + n0 + r] = f2bf(acc[i] * scale); }
}

__global__ __launch_bounds__(448) void attn_simple(const unsigned short* __restrict__ Q, const unsigned short* __restrict__ K0, const unsigned short* __restrict__ V0,
                            const unsigned short* __restrict__ K1, const unsigned short* __restrict__ V1, unsigned short* __restrict__ O) {
  __shared__ __attribute__((aligned(16))) unsigned short Vt[128][32 + 8];
  int u = blockIdx.x, br = u >> 8, b = (u >> 5) & 7, hh = u & 31;
  const unsigned short* K = (br ? K1 : K0) + (long)(b * H + hh) * SK * DH;
  const unsigned short* V = (br ? V1 : V0) + (long)(b * H + hh) * SK * DH;
  int wid = threadIdx.x >> 6, lane = threadIdx.x & 63, r = lane & 31, h = lane >> 5;
  int qrow = wid * 32 + r; int qc = qrow < SQ ? qrow : SQ - 1;
  const unsigned short* Qp = Q + ((long)br * M + b * SQ + qc) * HD + hh * DH;
  bf16x8 q[8];
  for (int s = 0; s < 8; ++s) q[s] = *(const bf16x8*)(Qp + 16 * s + 8 * h);
  f32x16 o[4] = {};
  float m = -1e30f, l = 0.f;
  for (int k0 = 0; k0 < SK; k0 += 32) {
    __syncthreads();
    for (int i = threadIdx.x; i < 32 * 128; i += 448) { int kk = i >> 7, d = i & 127; Vt[d][kk] = V[(long)(k0 + kk) * DH + d]; }
    __syncthreads();
    f32x16 s = {};
    for (int t = 0; t < 8; ++t) {
      bf16x8 a = *(const bf16x8*)(K + (long)(k0 + r) * DH + 16 * t + 8 * h);
      s = __builtin_amdgcn_mfma_f32_32x32x16_bf16(a, q[t], s, 0, 0, 0);
    }
    float mx = s[0]; for (int i = 1; i < 16; ++i) mx = fmaxf(mx, s[i]);
    mx = fmaxf(mx, __shfl_xor(mx, 32));
    float mn = fmaxf(m, mx), al = exp2f(m - mn); m = mn;
    float ps = 0; for (int i = 0; i < 16; ++i) { s[i] = exp2f(s[i] - mn); ps += s[i]; }
    ps += __shfl_xor(ps, 32); l = l * al + ps;
    for (int d = 0; d < 4; ++d) for (int i = 0; i < 16; ++i) o[d][i] *= al;
    for (int ks = 0; ks < 2; ++ks) {
      bf16x8 p; for (int j = 0; j < 8; ++j) p[j] = f2bf(s[8 * ks + j]);
      for (int d = 0; d < 4; ++d) {
        bf16x8 a;
        s16x4 lo = *(const s16x4*)(&Vt[32 * d + r][16 * ks + 4 * h]), hi = *(const s16x4*)(&Vt[32 * d + r][16 * ks + 8 + 4 * h]);
        a = __builtin_shufflevector(lo, hi, 0, 1, 2, 3, 4, 5, 6, 7);
        o[d] = __builtin_amdgcn_mfma_f32_32x32x16_bf16(a, p, o[d], 0, 0, 0);
      }
    }
  }
  if (qrow < SQ) {
    float il = 1.f / l;
    unsigned short* Op = O + ((long)br * M + b * SQ + qrow) * HD + hh * DH;
    for (int d = 0; d < 4; ++d) for (int i = 0; i < 16; ++i) { int dd = 32 * d + (i & 3) + 8 * (i >> 2) + 4 * h; Op[dd] = f2bf(o[d][i] * il); }
  }
}

extern "C" void kernel_launch(void* const* d_in, const int* in_sizes, int n_in, void* d_out, int out_size, void* d_ws, size_t ws_size, hipStream_t stream) {
  const unsigned short* xA = (const unsigned short*)d_in[0]; const unsigned short* kA = (const unsigned short*)d_in[1]; const unsigned short* vA = (const unsigned short*)d_in[2];
  const unsigned short* wqA = (const unsigned short*)d_in[3]; const unsigned short* woA = (const unsigned short*)d_in[4];
  const unsigned short* xB = (const unsigned short*)d_in[5]; const unsigned short* kB = (const unsigned short*)d_in[6]; const unsigned short* vB = (const unsigned short*)d_in[7];
  const unsigned short* wqB = (const unsigned short*)d_in[8]; const unsigned short* woB = (const unsigned short*)d_in[9];
  unsigned short* Q = (unsigned short*)d_ws; unsigned short* O = Q + (size_t)2 * M * HD; unsigned short* out = (unsigned short*)d_out;
  const float C = 0.12751743082f;
  gemm_simple<<<dim3(HD / 32, M / 128, 2), 256, 0, stream>>>(xA, wqA, Q, xB, wqB, Q + (size_t)M * HD, M, HD, DM, DM, HD, HD, C);
  attn_simple<<<512, 448, 0, stream>>>(Q, kA, vA, kB, vB, O);
  gemm_simple<<<dim3(DM / 32, M / 128, 2), 256, 0, stream>>>(O, woA, out, O + (size_t)M * HD, woB, out + DM, M, DM, HD, HD, DM, 2 * DM, 1.f);
}
```

```cpp
#include <hip/hip_runtime.h>
#include <hip/hip_bf16.h>

typedef __attribute__((ext_vector_type(8))) short bf16x8;
typedef __attribute__((ext_vector_type(4))) float f32x4;
typedef __attribute__((ext_vector_type(4))) unsigned u32x4;
typedef __attribute__((ext_vector_type(2))) float f32x2;
typedef __attribute__((ext_vector_type(2))) __bf16 bf16x2;
typedef unsigned short u16;

constexpr int SQ = 208, DM = 640, NH = 32, SK = 1024, DH = 128;
constexpr unsigned OBR_BYTES = 128u * 104u * 1024u;

__device__ __forceinline__ unsigned cvtpk(float lo, float hi) {
  f32x2 v = {lo, hi}; bf16x2 b = __builtin_convertvector(v, bf16x2); return __builtin_bit_cast(unsigned, b);
}
#define MFMA16(a, b, c) __builtin_amdgcn_mfma_f32_16x16x32_bf16((a), (b), (c), 0, 0, 0)

constexpr size_t WS_QS = 2u * (size_t)OBR_BYTES;
constexpr size_t WS_XC = WS_QS + 1024u * 28672u;
constexpr size_t WS_FLAGS = WS_XC + 1024u * 18432u;
typedef unsigned long long u64;
#define GAS __attribute__((address_space(1)))

constexpr unsigned WT_N = 6u;

struct Geo {
  unsigned br, b, j, h, r, kh, ng, rb0, nrb;
  __device__ __forceinline__ Geo(unsigned bid, unsigned w) {
    const unsigned xcd = bid & 7u; r = bid >> 3;
    br = xcd >> 2;
    b = r & 7u; j = 4u * (xcd & 3u) + (r >> 3);
    h = 2u * j + (w >> 1);
    const unsigned rg = r / 5u;
    kh = (xcd >> 1) & 1u; ng = r - rg * 5u; nrb = rg >= 4u ? 8u : 9u; rb0 = 52u * (xcd & 1u) + 9u * rg - (rg == 5u ? 1u : 0u);
  }
};
typedef const __attribute__((address_space(4))) u64* kargs_t;

__global__ __launch_bounds__(256, 1) void fused_block(const u16* xA, const u16* kA, const u16* vA, const u16* wqA, const u16* woA,
                                                      const u16* xB, const u16* kB, const u16* vB, const u16* wqB, const u16* woB, char* ws_, u16* out_) {
  kargs_t ka = (kargs_t)__builtin_amdgcn_kernarg_segment_ptr();
  unsigned bid = blockIdx.x, w = __builtin_amdgcn_readfirstlane(threadIdx.x >> 6);
  unsigned flag; u64 epoch, wsp, wop, outp;
  {
    const Geo c(bid, w);
    asm volatile(
".macro MM d,a,b,c\n"
"v_mfma_f32_16x16x32_bf16 \\d,\\a,\\b,\\c\n"
".endm\n"
".macro CV d,a,b\n"
"v_cvt_pk_bf16_f32 \\d,\\a,\\b\n"
".endm\n"
".macro TR d,a,o\n"
"ds_read_b64_tr_b16 \\d,\\a \\o\n"
".endm\n"
"s_memtime s[56:57]\ns_memrealtime s[58:59]\ns_load_dwordx8 s[48:55],%[ka],%[ko]\ns_load_dwordx2 %[wo],%[ka],%[ko] offset:0x20\n"
"s_load_dwordx2 %[ws],%[ka],0x50\ns_load_dwordx2 %[out],%[ka],0x58\ns_mov_b32 s60,%[w]\ns_mov_b32 s88,%[krot]\ns_and_b32 s84,s60,1\n"
"s_lshl_b32 s92,s60,10\ns_lshl_b32 s64,s60,11\ns_add_u32 s64,s64,45056\ns_add_u32 s91,s64,16384\ns_add_u32 s65,s92,77824\ns_lshl_b32 s66,s84,10\n"
"s_add_u32 s66,s66,102400\ns_lshl_b32 s86,s88,19\ns_lshl_b32 s80,s60,16\ns_add_u32 s86,s86,s80\ns_add_u32 s87,s86,262144\ns_mul_i32 s67,s60,10240\n"
"s_add_u32 s68,s67,40960\ns_add_u32 s69,s67,81920\ns_add_u32 s70,s67,122880\ns_add_u32 s71,s67,163840\ns_add_u32 s72,s67,204800\n"
"s_mul_i32 s73,s84,10240\ns_add_u32 s73,s73,245760\ns_mov_b32 s89,59392\nv_mbcnt_lo_u32_b32 v24,-1,0\nv_mbcnt_hi_u32_b32 v24,-1,v24\n"
"v_and_b32 v28,3,v24\nv_bfe_u32 v29,v24,2,3\nv_lshrrev_b32 v30,5,v24\nv_lshlrev_b32 v31,13,v29\nv_lshl_add_u32 v31,v30,6,v31\n"
"v_lshl_add_u32 v13,v28,4,v31\nv_lshrrev_b32 v26,4,v24\ns_lshl_b32 s80,s84,2\nv_xor_b32 v31,s80,v26\nv_and_b32 v32,7,v24\nv_xor_b32 v31,v31,v32\n"
"v_lshrrev_b32 v32,3,v24\nv_mul_u32_u24 v32,1280,v32\nv_lshl_add_u32 v31,v31,4,v32\nv_lshl_add_u32 v12,s88,7,v31\ns_sub_u32 s46,5242880,%[oq]\n"
"s_mov_b32 s47,0x20000\ns_mov_b32 s42,266240\ns_mov_b32 s43,0x20000\ns_getpc_b64 s[82:83]\nv_lshlrev_b32 v33,6,v24\nv_lshl_add_u32 v33,s60,12,v33\n"
"global_load_dword a143,v33,s[82:83]\nv_add_u32 v33,0x4000,v33\ns_waitcnt lgkmcnt(0)\ns_add_u32 s44,s54,%[oq]\ns_addc_u32 s45,s55,0\n"
"s_and_b32 s45,s45,0xffff\ns_add_u32 s40,s48,%[ox]\ns_addc_u32 s41,s49,0\ns_and_b32 s41,s41,0xffff\ns_add_u32 m0,s64,0\ns_nop 0\n"
"buffer_load_dwordx4 v13,s[44:47],s86 offen offset:0 lds\ns_add_u32 m0,s64,896\ns_nop 0\nbuffer_load_dwordx4 v13,s[44:47],s86 offen offset:128 lds\n"
"s_add_u32 m0,s64,7936\ns_nop 0\nbuffer_load_dwordx4 v13,s[44:47],s86 offen offset:256 lds\ns_add_u32 m0,s64,8832\ns_nop 0\n"
"buffer_load_dwordx4 v13,s[44:47],s86 offen offset:384 lds\ns_add_u32 m0,s65,0\ns_nop 0\nbuffer_load_dwordx4 v12,s[40:43],s67 offen offset:0 lds\n"
"s_add_u32 m0,s65,4096\ns_nop 0\nbuffer_load_dwordx4 v12,s[40:43],s68 offen offset:0 lds\ns_add_u32 m0,s65,8192\ns_nop 0\n"
"buffer_load_dwordx4 v12,s[40:43],s69 offen offset:0 lds\ns_add_u32 m0,s65,12288\ns_nop 0\nbuffer_load_dwordx4 v12,s[40:43],s70 offen offset:0 lds\n"
"s_add_u32 m0,s65,16384\ns_nop 0\nbuffer_load_dwordx4 v12,s[40:43],s71 offen offset:0 lds\ns_add_u32 m0,s65,20480\ns_nop 0\n"
"buffer_load_dwordx4 v12,s[40:43],s72 offen offset:0 lds\ns_mov_b32 m0,s66\ns_nop 0\nbuffer_load_dwordx4 v12,s[40:43],s73 offen offset:0 lds\n"
"s_add_u32 m0,s91,0\ns_nop 0\nbuffer_load_dwordx4 v13,s[44:47],s87 offen offset:0 lds\ns_add_u32 m0,s91,896\ns_nop 0\n"
"buffer_load_dwordx4 v13,s[44:47],s87 offen offset:128 lds\ns_add_u32 m0,s91,7936\ns_nop 0\nbuffer_load_dwordx4 v13,s[44:47],s87 offen offset:256 lds\n"
"s_add_u32 m0,s91,8832\ns_nop 0\nbuffer_load_dwordx4 v13,s[44:47],s87 offen offset:384 lds\ns_add_u32 s87,s86,262144\ns_add_u32 s91,s64,16384\n"
"s_add_u32 s88,s88,1\ns_cmp_eq_u32 s88,10\ns_cselect_b32 s80,-9,1\ns_cselect_b32 s88,0,s88\nv_lshl_add_u32 v12,s80,7,v12\ns_lshl_b32 s80,s80,19\n"
"s_add_u32 s86,s86,s80\ns_add_u32 s64,s64,s89\ns_add_u32 s65,s65,s89\ns_add_u32 s66,s66,s89\ns_sub_u32 s89,0,s89\ns_add_u32 m0,s64,0\ns_nop 0\n"
"buffer_load_dwordx4 v13,s[44:47],s86 offen offset:0 lds\ns_add_u32 m0,s64,896\ns_nop 0\nbuffer_load_dwordx4 v13,s[44:47],s86 offen offset:128 lds\n"
"s_add_u32 m0,s64,7936\ns_nop 0\nbuffer_load_dwordx4 v13,s[44:47],s86 offen offset:256 lds\ns_add_u32 m0,s64,8832\ns_nop 0\n"
"buffer_load_dwordx4 v13,s[44:47],s86 offen offset:384 lds\ns_add_u32 m0,s65,0\ns_nop 0\nbuffer_load_dwordx4 v12,s[40:43],s67 offen offset:0 lds\n"
"s_add_u32 m0,s65,4096\ns_nop 0\nbuffer_load_dwordx4 v12,s[40:43],s68 offen offset:0 lds\ns_add_u32 m0,s65,8192\ns_nop 0\n"
"buffer_load_dwordx4 v12,s[40:43],s69 offen offset:0 lds\ns_add_u32 m0,s65,12288\ns_nop 0\nbuffer_load_dwordx4 v12,s[40:43],s70 offen offset:0 lds\n"
"s_add_u32 m0,s65,16384\ns_nop 0\nbuffer_load_dwordx4 v12,s[40:43],s71 offen offset:0 lds\ns_add_u32 m0,s65,20480\ns_nop 0\n"
"buffer_load_dwordx4 v12,s[40:43],s72 offen offset:0 lds\ns_mov_b32 m0,s66\ns_nop 0\nbuffer_load_dwordx4 v12,s[40:43],s73 offen offset:0 lds\n"
"s_add_u32 s87,s86,262144\ns_add_u32 s91,s64,16384\ns_add_u32 s88,s88,1\ns_cmp_eq_u32 s88,10\ns_cselect_b32 s80,-9,1\ns_cselect_b32 s88,0,s88\n"
"v_lshl_add_u32 v12,s80,7,v12\ns_lshl_b32 s80,s80,19\ns_add_u32 s86,s86,s80\ns_add_u32 s64,s64,s89\ns_add_u32 s65,s65,s89\ns_add_u32 s66,s66,s89\n"
"s_sub_u32 s89,0,s89\ns_mov_b32 s80,75499520\ns_load_dwordx2 %[ep],%[ws],s80\ns_add_u32 s48,s50,%[okv]\ns_addc_u32 s49,s51,0\ns_and_b32 s49,s49,0xffff\n"
"s_mov_b32 s50,262144\ns_mov_b32 s51,0x20000\ns_add_u32 s52,s52,%[okv]\ns_addc_u32 s53,s53,0\ns_sub_u32 s52,s52,1024\ns_subb_u32 s53,s53,0\n"
"s_and_b32 s53,s53,0xffff\ns_mov_b32 s54,263168\ns_mov_b32 s55,0x20000\ns_lshr_b32 s85,s60,1\ns_lshl_b32 s63,s60,13\ns_mov_b32 s61,%[rot]\n"
"s_lshl_b32 s80,s84,1\ns_add_u32 s61,s61,s80\ns_lshl_b32 s61,s61,12\ns_and_b32 s61,s61,0x3ffff\ns_mov_b32 s62,s61\ns_mov_b32 s78,%[ooff]\n"
"s_mov_b32 s74,0x3e0293ee\ns_mov_b32 s75,0x3e0293ee\ns_mov_b32 s79,0x67800000\ns_mov_b32 s90,8\nv_and_b32 v25,15,v24\nv_bfe_u32 v27,v24,2,2\n"
"v_lshrrev_b32 v31,1,v25\nv_xor_b32 v31,v31,v26\nv_lshlrev_b32 v31,4,v31\nv_lshl_add_u32 v31,v25,7,v31\nv_add_u32 v16,102400,v31\n"
"s_mul_i32 s80,s84,12288\ns_add_u32 s80,s80,77824\nv_add_u32 v14,s80,v31\nv_xor_b32 v17,64,v16\nv_xor_b32 v15,64,v14\nv_lshlrev_b32 v31,11,v26\n"
"v_lshl_add_u32 v31,v27,6,v31\nv_lshl_add_u32 v31,v28,4,v31\ns_lshl_b32 s80,s85,13\ns_add_u32 s80,s80,45056\ns_lshl_b32 s81,s84,10\n"
"v_add_u32 v31,s80,v31\nv_add_u32 v18,s81,v31\nv_subrev_u32 v19,s81,v31\nv_xor_b32 v31,v25,v26\nv_lshlrev_b32 v31,4,v31\nv_lshl_add_u32 v7,v26,8,v31\n"
"v_xor_b32 v8,64,v7\nv_xor_b32 v9,128,v7\nv_xor_b32 v10,192,v7\nv_lshl_add_u32 v31,v25,8,v31\ns_lshl_b32 s80,s84,7\nv_xor_b32 v31,s80,v31\n"
"v_add_u32 v1,s63,v31\nv_xor_b32 v2,64,v1\nv_xor_b32 v3,128,v1\nv_xor_b32 v4,192,v1\nv_xor_b32 v31,v28,v26\nv_lshlrev_b32 v31,4,v31\n"
"v_lshl_add_u32 v31,v28,8,v31\nv_xor_b32 v31,s80,v31\nv_lshl_add_u32 v20,s85,14,v31\nv_xor_b32 v21,64,v20\nv_xor_b32 v22,128,v20\nv_xor_b32 v23,192,v20\n"
"v_and_b32 v32,1,v26\nv_lshlrev_b32 v31,11,v30\nv_lshl_add_u32 v31,v29,6,v31\nv_lshl_add_u32 v31,v32,5,v31\nv_lshl_add_u32 v31,v28,3,v31\n"
"s_add_u32 s80,s63,32768\nv_add_u32 v5,s80,v31\nv_xor_b32 v6,32,v5\nv_lshlrev_b32 v32,1,v32\nv_xor_b32 v32,v32,v28\nv_lshlrev_b32 v31,8,v29\n"
"v_lshl_add_u32 v31,v30,6,v31\nv_lshl_add_u32 v31,v32,4,v31\nv_add_u32 v11,1024,v31\nv_add_u32 v0,128,v31\nv_lshlrev_b32 v31,4,v24\n"
"v_lshl_add_u32 v36,s92,1,v31\nv_xor_b32 v37,2048,v36\nv_add_u32 v36,104448,v36\nv_add_u32 v37,104448,v37\nv_mov_b32 v40,0\nv_mov_b32 v41,0\n"
"v_mov_b32 v42,0\nv_mov_b32 v43,0\ns_nop 1\nv_mfma_f32_32x32x16_bf16 v[48:63],v[40:43],v[40:43],0\n"
"v_mfma_f32_32x32x16_bf16 v[64:79],v[40:43],v[40:43],0\nv_mfma_f32_32x32x16_bf16 v[80:95],v[40:43],v[40:43],0\n"
"v_mfma_f32_32x32x16_bf16 v[96:111],v[40:43],v[40:43],0\nv_mfma_f32_32x32x16_bf16 v[112:127],v[40:43],v[40:43],0\n"
"v_mfma_f32_32x32x16_bf16 v[128:143],v[40:43],v[40:43],0\nv_mfma_f32_32x32x16_bf16 v[144:159],v[40:43],v[40:43],0\n"
"v_mfma_f32_32x32x16_bf16 v[160:175],v[40:43],v[40:43],0\nv_mfma_f32_32x32x16_bf16 v[176:191],v[40:43],v[40:43],0\n"
"v_mfma_f32_32x32x16_bf16 v[192:207],v[40:43],v[40:43],0\nv_mfma_f32_32x32x16_bf16 v[208:223],v[40:43],v[40:43],0\n"
"v_mfma_f32_32x32x16_bf16 v[224:239],v[40:43],v[40:43],0\nv_mfma_f32_32x32x16_bf16 v[240:255],v[40:43],v[40:43],0\ns_getpc_b64 s[82:83]\n"
"s_add_u32 s82,s82,s92\ns_addc_u32 s83,s83,0\nglobal_load_dword a143,v7,s[82:83]\ns_add_u32 s82,s82,4096\ns_addc_u32 s83,s83,0\n"
"global_load_dword a143,v7,s[82:83]\ns_add_u32 s82,s82,4096\ns_addc_u32 s83,s83,0\nglobal_load_dword a143,v7,s[82:83]\ns_add_u32 s82,s82,4096\n"
"s_addc_u32 s83,s83,0\nglobal_load_dword a143,v7,s[82:83]\ns_add_u32 s82,s82,4096\ns_addc_u32 s83,s83,0\nglobal_load_dword a143,v7,s[82:83]\n"
"s_add_u32 s82,s82,4096\ns_addc_u32 s83,s83,0\nglobal_load_dword a143,v7,s[82:83]\ns_add_u32 s82,s82,4096\ns_addc_u32 s83,s83,0\n"
"global_load_dword a143,v7,s[82:83]\ns_add_u32 s82,s82,4096\ns_addc_u32 s83,s83,0\nglobal_load_dword a143,v7,s[82:83]\ns_add_u32 s82,s82,4096\n"
"s_addc_u32 s83,s83,0\ns_waitcnt vmcnt(23)\ns_memtime s[76:77]\ns_memrealtime vcc\ns_waitcnt lgkmcnt(0)\ns_barrier\ns_sub_u32 s76,s76,s56\n"
"s_sub_u32 s77,vcc_lo,s58\ns_mul_i32 s76,s76,10\ns_mul_i32 s77,s77,200\ns_cmp_gt_u32 s76,s77\ns_cselect_b32 s93,249,64\ns_cselect_b32 s77,8,60\n"
"ds_read_b128 a[24:27],v16 offset:0\nTR a[28:29],v18 offset:0\nTR a[30:31],v18 offset:256\nTR a[32:33],v18 offset:8\nTR a[34:35],v18 offset:264\n"
"TR a[36:37],v18 offset:512\nTR a[38:39],v18 offset:768\nTR a[40:41],v18 offset:520\nTR a[42:43],v18 offset:776\nds_read_b128 a[0:3],v14 offset:0\n"
"TR a[44:45],v19 offset:1024\nTR a[46:47],v19 offset:1280\nTR a[48:49],v19 offset:1032\nTR a[50:51],v19 offset:1288\nTR a[52:53],v19 offset:1536\n"
"TR a[54:55],v19 offset:1792\nTR a[56:57],v19 offset:1544\nTR a[58:59],v19 offset:1800\nds_read_b128 a[4:7],v14 offset:2048\n"
"ds_read_b128 a[8:11],v14 offset:4096\nds_read_b128 a[12:15],v14 offset:6144\nds_read_b128 a[16:19],v14 offset:8192\n"
"ds_read_b128 a[20:23],v14 offset:10240\ns_nop 7\ns_nop 7\n.p2align 6\nL_q_%=:\ns_waitcnt vmcnt(11) lgkmcnt(0)\ns_barrier\n"
"MM v[48:51],a[28:31],a[24:27],v[48:51]\nds_read_b128 a[84:87],v17 offset:0\nMM v[52:55],a[32:35],a[24:27],v[52:55]\ns_add_u32 m0,s91,0\n"
"MM v[56:59],a[36:39],a[24:27],v[56:59]\nTR a[88:89],v18 offset:16384\nMM v[60:63],a[40:43],a[24:27],v[60:63]\n"
"buffer_load_dwordx4 v13,s[44:47],s87 offen offset:0 lds\nMM v[64:67],a[28:31],a[0:3],v[64:67]\nTR a[90:91],v18 offset:16640\n"
"MM v[68:71],a[32:35],a[0:3],v[68:71]\nMM v[72:75],a[36:39],a[0:3],v[72:75]\nTR a[92:93],v18 offset:16392\ns_add_u32 m0,s91,896\n"
"MM v[76:79],a[40:43],a[0:3],v[76:79]\nMM v[80:83],a[44:47],a[0:3],v[80:83]\nTR a[94:95],v18 offset:16648\nMM v[84:87],a[48:51],a[0:3],v[84:87]\n"
"buffer_load_dwordx4 v13,s[44:47],s87 offen offset:128 lds\nMM v[88:91],a[52:55],a[0:3],v[88:91]\nTR a[96:97],v18 offset:16896\n"
"MM v[92:95],a[56:59],a[0:3],v[92:95]\ns_add_u32 m0,s91,7936\nMM v[96:99],a[28:31],a[4:7],v[96:99]\nTR a[98:99],v18 offset:17152\n"
"MM v[100:103],a[32:35],a[4:7],v[100:103]\nMM v[104:107],a[36:39],a[4:7],v[104:107]\nTR a[100:101],v18 offset:16904\n"
"MM v[108:111],a[40:43],a[4:7],v[108:111]\nbuffer_load_dwordx4 v13,s[44:47],s87 offen offset:256 lds\nMM v[112:115],a[44:47],a[4:7],v[112:115]\n"
"TR a[102:103],v18 offset:17160\nMM v[116:119],a[48:51],a[4:7],v[116:119]\ns_add_u32 m0,s91,8832\nMM v[120:123],a[52:55],a[4:7],v[120:123]\n"
"ds_read_b128 a[60:63],v15 offset:0\nMM v[124:127],a[56:59],a[4:7],v[124:127]\nbuffer_load_dwordx4 v13,s[44:47],s87 offen offset:384 lds\n"
"MM v[128:131],a[28:31],a[8:11],v[128:131]\nTR a[104:105],v19 offset:17408\nMM v[132:135],a[32:35],a[8:11],v[132:135]\n"
"MM v[136:139],a[36:39],a[8:11],v[136:139]\nTR a[106:107],v19 offset:17664\ns_add_u32 m0,s64,0\nMM v[140:143],a[40:43],a[8:11],v[140:143]\n"
"MM v[144:147],a[44:47],a[8:11],v[144:147]\nTR a[108:109],v19 offset:17416\nMM v[148:151],a[48:51],a[8:11],v[148:151]\n"
"buffer_load_dwordx4 v13,s[44:47],s86 offen offset:0 lds\nMM v[152:155],a[52:55],a[8:11],v[152:155]\nTR a[110:111],v19 offset:17672\n"
"MM v[156:159],a[56:59],a[8:11],v[156:159]\ns_add_u32 m0,s64,896\nMM v[160:163],a[28:31],a[12:15],v[160:163]\nTR a[112:113],v19 offset:17920\n"
"MM v[164:167],a[32:35],a[12:15],v[164:167]\nMM v[168:171],a[36:39],a[12:15],v[168:171]\nTR a[114:115],v19 offset:18176\n"
"MM v[172:175],a[40:43],a[12:15],v[172:175]\nbuffer_load_dwordx4 v13,s[44:47],s86 offen offset:128 lds\nMM v[176:179],a[44:47],a[12:15],v[176:179]\n"
"TR a[116:117],v19 offset:17928\nMM v[180:183],a[48:51],a[12:15],v[180:183]\ns_add_u32 m0,s64,7936\nMM v[184:187],a[52:55],a[12:15],v[184:187]\n"
"TR a[118:119],v19 offset:18184\nMM v[188:191],a[56:59],a[12:15],v[188:191]\nbuffer_load_dwordx4 v13,s[44:47],s86 offen offset:256 lds\n"
"MM v[192:195],a[28:31],a[16:19],v[192:195]\nds_read_b128 a[64:67],v15 offset:2048\nMM v[196:199],a[32:35],a[16:19],v[196:199]\n"
"MM v[200:203],a[36:39],a[16:19],v[200:203]\nds_read_b128 a[68:71],v15 offset:4096\ns_add_u32 m0,s64,8832\nMM v[204:207],a[40:43],a[16:19],v[204:207]\n"
"MM v[208:211],a[44:47],a[16:19],v[208:211]\nds_read_b128 a[72:75],v15 offset:6144\nMM v[212:215],a[48:51],a[16:19],v[212:215]\n"
"buffer_load_dwordx4 v13,s[44:47],s86 offen offset:384 lds\nMM v[216:219],a[52:55],a[16:19],v[216:219]\nds_read_b128 a[76:79],v15 offset:8192\n"
"v_add_u32 v18,s89,v18\nMM v[220:223],a[56:59],a[16:19],v[220:223]\nv_add_u32 v16,s89,v16\nMM v[224:227],a[28:31],a[20:23],v[224:227]\n"
"ds_read_b128 a[80:83],v15 offset:10240\nMM v[228:231],a[32:35],a[20:23],v[228:231]\nv_add_u32 v19,s89,v19\nMM v[232:235],a[36:39],a[20:23],v[232:235]\n"
"v_add_u32 v14,s89,v14\nMM v[236:239],a[40:43],a[20:23],v[236:239]\nMM v[240:243],a[44:47],a[20:23],v[240:243]\nv_add_u32 v15,s89,v15\n"
"MM v[244:247],a[48:51],a[20:23],v[244:247]\nv_add_u32 v17,s89,v17\nMM v[248:251],a[52:55],a[20:23],v[248:251]\n"
"MM v[252:255],a[56:59],a[20:23],v[252:255]\ns_waitcnt vmcnt(8) lgkmcnt(0)\ns_barrier\nMM v[48:51],a[88:91],a[84:87],v[48:51]\n"
"ds_read_b128 a[24:27],v16 offset:0\nMM v[52:55],a[92:95],a[84:87],v[52:55]\ns_add_u32 m0,s65,0\nMM v[56:59],a[96:99],a[84:87],v[56:59]\n"
"TR a[28:29],v18 offset:0\nMM v[60:63],a[100:103],a[84:87],v[60:63]\nbuffer_load_dwordx4 v12,s[40:43],s67 offen offset:0 lds\n"
"MM v[64:67],a[88:91],a[60:63],v[64:67]\nTR a[30:31],v18 offset:256\nMM v[68:71],a[92:95],a[60:63],v[68:71]\ns_add_u32 m0,s65,4096\n"
"MM v[72:75],a[96:99],a[60:63],v[72:75]\nTR a[32:33],v18 offset:8\nMM v[76:79],a[100:103],a[60:63],v[76:79]\nMM v[80:83],a[104:107],a[60:63],v[80:83]\n"
"TR a[34:35],v18 offset:264\nMM v[84:87],a[108:111],a[60:63],v[84:87]\nbuffer_load_dwordx4 v12,s[40:43],s68 offen offset:0 lds\n"
"MM v[88:91],a[112:115],a[60:63],v[88:91]\nTR a[36:37],v18 offset:512\ns_add_u32 m0,s65,8192\nMM v[92:95],a[116:119],a[60:63],v[92:95]\n"
"MM v[96:99],a[88:91],a[64:67],v[96:99]\nTR a[38:39],v18 offset:768\nMM v[100:103],a[92:95],a[64:67],v[100:103]\n"
"buffer_load_dwordx4 v12,s[40:43],s69 offen offset:0 lds\nMM v[104:107],a[96:99],a[64:67],v[104:107]\nTR a[40:41],v18 offset:520\n"
"MM v[108:111],a[100:103],a[64:67],v[108:111]\ns_add_u32 m0,s65,12288\nMM v[112:115],a[104:107],a[64:67],v[112:115]\nTR a[42:43],v18 offset:776\n"
"MM v[116:119],a[108:111],a[64:67],v[116:119]\nMM v[120:123],a[112:115],a[64:67],v[120:123]\nds_read_b128 a[0:3],v14 offset:0\n"
"MM v[124:127],a[116:119],a[64:67],v[124:127]\nbuffer_load_dwordx4 v12,s[40:43],s70 offen offset:0 lds\nMM v[128:131],a[88:91],a[68:71],v[128:131]\n"
"TR a[44:45],v19 offset:1024\ns_add_u32 m0,s65,16384\nMM v[132:135],a[92:95],a[68:71],v[132:135]\nMM v[136:139],a[96:99],a[68:71],v[136:139]\n"
"TR a[46:47],v19 offset:1280\nMM v[140:143],a[100:103],a[68:71],v[140:143]\nbuffer_load_dwordx4 v12,s[40:43],s71 offen offset:0 lds\n"
"MM v[144:147],a[104:107],a[68:71],v[144:147]\nTR a[48:49],v19 offset:1032\nMM v[148:151],a[108:111],a[68:71],v[148:151]\ns_add_u32 m0,s65,20480\n"
"MM v[152:155],a[112:115],a[68:71],v[152:155]\nTR a[50:51],v19 offset:1288\nMM v[156:159],a[116:119],a[68:71],v[156:159]\n"
"buffer_load_dwordx4 v12,s[40:43],s72 offen offset:0 lds\nMM v[160:163],a[88:91],a[72:75],v[160:163]\nTR a[52:53],v19 offset:1536\n"
"MM v[164:167],a[92:95],a[72:75],v[164:167]\nMM v[168:171],a[96:99],a[72:75],v[168:171]\nTR a[54:55],v19 offset:1792\ns_mov_b32 m0,s66\n"
"MM v[172:175],a[100:103],a[72:75],v[172:175]\nMM v[176:179],a[104:107],a[72:75],v[176:179]\nTR a[56:57],v19 offset:1544\n"
"MM v[180:183],a[108:111],a[72:75],v[180:183]\nbuffer_load_dwordx4 v12,s[40:43],s73 offen offset:0 lds\nMM v[184:187],a[112:115],a[72:75],v[184:187]\n"
"TR a[58:59],v19 offset:1800\nMM v[188:191],a[116:119],a[72:75],v[188:191]\ns_add_u32 s87,s86,262144\ns_add_u32 s91,s64,16384\n"
"MM v[192:195],a[88:91],a[76:79],v[192:195]\nds_read_b128 a[4:7],v14 offset:2048\nMM v[196:199],a[92:95],a[76:79],v[196:199]\ns_add_u32 s88,s88,1\n"
"s_cmp_eq_u32 s88,10\ns_cselect_b32 s80,-9,1\ns_cselect_b32 s88,0,s88\nMM v[200:203],a[96:99],a[76:79],v[200:203]\nds_read_b128 a[8:11],v14 offset:4096\n"
"MM v[204:207],a[100:103],a[76:79],v[204:207]\nMM v[208:211],a[104:107],a[76:79],v[208:211]\nds_read_b128 a[12:15],v14 offset:6144\n"
"MM v[212:215],a[108:111],a[76:79],v[212:215]\nv_lshl_add_u32 v12,s80,7,v12\ns_lshl_b32 s80,s80,19\nMM v[216:219],a[112:115],a[76:79],v[216:219]\n"
"ds_read_b128 a[16:19],v14 offset:8192\nMM v[220:223],a[116:119],a[76:79],v[220:223]\ns_add_u32 s86,s86,s80\ns_add_u32 s64,s64,s89\n"
"MM v[224:227],a[88:91],a[80:83],v[224:227]\nds_read_b128 a[20:23],v14 offset:10240\nMM v[228:231],a[92:95],a[80:83],v[228:231]\ns_add_u32 s65,s65,s89\n"
"s_add_u32 s66,s66,s89\nMM v[232:235],a[96:99],a[80:83],v[232:235]\nMM v[236:239],a[100:103],a[80:83],v[236:239]\ns_sub_u32 s89,0,s89\n"
"MM v[240:243],a[104:107],a[80:83],v[240:243]\nMM v[244:247],a[108:111],a[80:83],v[244:247]\nMM v[248:251],a[112:115],a[80:83],v[248:251]\n"
"MM v[252:255],a[116:119],a[80:83],v[252:255]\ns_sub_u32 s90,s90,1\ns_cmp_lg_u32 s90,0\ns_cbranch_scc1 L_q_%=\ns_waitcnt vmcnt(11) lgkmcnt(0)\n"
"s_barrier\nMM v[48:51],a[28:31],a[24:27],v[48:51]\nds_read_b128 a[84:87],v17 offset:0\nMM v[52:55],a[32:35],a[24:27],v[52:55]\ns_add_u32 m0,s91,0\n"
"MM v[56:59],a[36:39],a[24:27],v[56:59]\nTR a[88:89],v18 offset:16384\nMM v[60:63],a[40:43],a[24:27],v[60:63]\n"
"buffer_load_dwordx4 v13,s[44:47],s87 offen offset:0 lds\nMM v[64:67],a[28:31],a[0:3],v[64:67]\nTR a[90:91],v18 offset:16640\ns_add_u32 m0,s91,896\n"
"MM v[68:71],a[32:35],a[0:3],v[68:71]\nMM v[72:75],a[36:39],a[0:3],v[72:75]\nTR a[92:93],v18 offset:16392\nMM v[76:79],a[40:43],a[0:3],v[76:79]\n"
"buffer_load_dwordx4 v13,s[44:47],s87 offen offset:128 lds\nMM v[80:83],a[44:47],a[0:3],v[80:83]\nTR a[94:95],v18 offset:16648\ns_add_u32 m0,s91,7936\n"
"MM v[84:87],a[48:51],a[0:3],v[84:87]\nMM v[88:91],a[52:55],a[0:3],v[88:91]\nTR a[96:97],v18 offset:16896\nMM v[92:95],a[56:59],a[0:3],v[92:95]\n"
"buffer_load_dwordx4 v13,s[44:47],s87 offen offset:256 lds\nMM v[96:99],a[28:31],a[4:7],v[96:99]\nTR a[98:99],v18 offset:17152\ns_add_u32 m0,s91,8832\n"
"MM v[100:103],a[32:35],a[4:7],v[100:103]\nMM v[104:107],a[36:39],a[4:7],v[104:107]\nTR a[100:101],v18 offset:16904\n"
"MM v[108:111],a[40:43],a[4:7],v[108:111]\nbuffer_load_dwordx4 v13,s[44:47],s87 offen offset:384 lds\nMM v[112:115],a[44:47],a[4:7],v[112:115]\n"
"TR a[102:103],v18 offset:17160\ns_add_u32 m0,s63,0\nMM v[116:119],a[48:51],a[4:7],v[116:119]\nMM v[120:123],a[52:55],a[4:7],v[120:123]\n"
"ds_read_b128 a[60:63],v15 offset:0\nMM v[124:127],a[56:59],a[4:7],v[124:127]\nbuffer_load_dwordx4 v7,s[48:51],s61 offen offset:0 nt lds\n"
"MM v[128:131],a[28:31],a[8:11],v[128:131]\nTR a[104:105],v19 offset:17408\nMM v[132:135],a[32:35],a[8:11],v[132:135]\n"
"buffer_load_dwordx4 v8,s[48:51],s61 offen offset:1024 nt lds\nMM v[136:139],a[36:39],a[8:11],v[136:139]\nTR a[106:107],v19 offset:17664\n"
"MM v[140:143],a[40:43],a[8:11],v[140:143]\nbuffer_load_dwordx4 v9,s[48:51],s61 offen offset:2048 nt lds\nMM v[144:147],a[44:47],a[8:11],v[144:147]\n"
"TR a[108:109],v19 offset:17416\nMM v[148:151],a[48:51],a[8:11],v[148:151]\nbuffer_load_dwordx4 v10,s[48:51],s61 offen offset:3072 nt lds\n"
"MM v[152:155],a[52:55],a[8:11],v[152:155]\nTR a[110:111],v19 offset:17672\ns_add_u32 s61,s61,4096\nMM v[156:159],a[56:59],a[8:11],v[156:159]\n"
"MM v[160:163],a[28:31],a[12:15],v[160:163]\nTR a[112:113],v19 offset:17920\ns_and_b32 s61,s61,0x3ffff\nMM v[164:167],a[32:35],a[12:15],v[164:167]\n"
"MM v[168:171],a[36:39],a[12:15],v[168:171]\nTR a[114:115],v19 offset:18176\ns_add_u32 m0,s63,4096\nMM v[172:175],a[40:43],a[12:15],v[172:175]\n"
"MM v[176:179],a[44:47],a[12:15],v[176:179]\nTR a[116:117],v19 offset:17928\nMM v[180:183],a[48:51],a[12:15],v[180:183]\n"
"buffer_load_dwordx4 v7,s[48:51],s61 offen offset:0 nt lds\nMM v[184:187],a[52:55],a[12:15],v[184:187]\nTR a[118:119],v19 offset:18184\n"
"MM v[188:191],a[56:59],a[12:15],v[188:191]\nbuffer_load_dwordx4 v8,s[48:51],s61 offen offset:1024 nt lds\nMM v[192:195],a[28:31],a[16:19],v[192:195]\n"
"ds_read_b128 a[64:67],v15 offset:2048\nMM v[196:199],a[32:35],a[16:19],v[196:199]\nbuffer_load_dwordx4 v9,s[48:51],s61 offen offset:2048 nt lds\n"
"MM v[200:203],a[36:39],a[16:19],v[200:203]\nds_read_b128 a[68:71],v15 offset:4096\nMM v[204:207],a[40:43],a[16:19],v[204:207]\n"
"buffer_load_dwordx4 v10,s[48:51],s61 offen offset:3072 nt lds\nMM v[208:211],a[44:47],a[16:19],v[208:211]\nds_read_b128 a[72:75],v15 offset:6144\n"
"s_add_u32 s61,s61,12288\nMM v[212:215],a[48:51],a[16:19],v[212:215]\nMM v[216:219],a[52:55],a[16:19],v[216:219]\nds_read_b128 a[76:79],v15 offset:8192\n"
"s_and_b32 s61,s61,0x3ffff\nMM v[220:223],a[56:59],a[16:19],v[220:223]\nv_add_u32 v18,s89,v18\nv_add_u32 v16,s89,v16\n"
"MM v[224:227],a[28:31],a[20:23],v[224:227]\nds_read_b128 a[80:83],v15 offset:10240\nMM v[228:231],a[32:35],a[20:23],v[228:231]\nv_add_u32 v19,s89,v19\n"
"MM v[232:235],a[36:39],a[20:23],v[232:235]\nv_add_u32 v14,s89,v14\nMM v[236:239],a[40:43],a[20:23],v[236:239]\n"
"MM v[240:243],a[44:47],a[20:23],v[240:243]\nv_add_u32 v15,s89,v15\nMM v[244:247],a[48:51],a[20:23],v[244:247]\nv_add_u32 v17,s89,v17\n"
"MM v[248:251],a[52:55],a[20:23],v[248:251]\nMM v[252:255],a[56:59],a[20:23],v[252:255]\ns_waitcnt vmcnt(12) lgkmcnt(0)\ns_barrier\n"
"MM v[48:51],a[88:91],a[84:87],v[48:51]\nds_read_b128 a[24:27],v16 offset:0\nMM v[52:55],a[92:95],a[84:87],v[52:55]\n"
"MM v[56:59],a[96:99],a[84:87],v[56:59]\nTR a[28:29],v18 offset:0\nMM v[60:63],a[100:103],a[84:87],v[60:63]\nMM v[64:67],a[88:91],a[60:63],v[64:67]\n"
"TR a[30:31],v18 offset:256\nMM v[68:71],a[92:95],a[60:63],v[68:71]\nMM v[72:75],a[96:99],a[60:63],v[72:75]\nTR a[32:33],v18 offset:8\n"
"MM v[76:79],a[100:103],a[60:63],v[76:79]\nMM v[80:83],a[104:107],a[60:63],v[80:83]\nTR a[34:35],v18 offset:264\n"
"MM v[84:87],a[108:111],a[60:63],v[84:87]\nMM v[88:91],a[112:115],a[60:63],v[88:91]\nTR a[36:37],v18 offset:512\n"
"MM v[92:95],a[116:119],a[60:63],v[92:95]\nMM v[96:99],a[88:91],a[64:67],v[96:99]\nTR a[38:39],v18 offset:768\n"
"MM v[100:103],a[92:95],a[64:67],v[100:103]\nMM v[104:107],a[96:99],a[64:67],v[104:107]\nTR a[40:41],v18 offset:520\n"
"MM v[108:111],a[100:103],a[64:67],v[108:111]\nMM v[112:115],a[104:107],a[64:67],v[112:115]\nTR a[42:43],v18 offset:776\n"
"MM v[116:119],a[108:111],a[64:67],v[116:119]\nMM v[120:123],a[112:115],a[64:67],v[120:123]\nds_read_b128 a[0:3],v14 offset:0\n"
"MM v[124:127],a[116:119],a[64:67],v[124:127]\nMM v[128:131],a[88:91],a[68:71],v[128:131]\nTR a[44:45],v19 offset:1024\n"
"MM v[132:135],a[92:95],a[68:71],v[132:135]\nMM v[136:139],a[96:99],a[68:71],v[136:139]\nTR a[46:47],v19 offset:1280\n"
"MM v[140:143],a[100:103],a[68:71],v[140:143]\nMM v[144:147],a[104:107],a[68:71],v[144:147]\nTR a[48:49],v19 offset:1032\n"
"MM v[148:151],a[108:111],a[68:71],v[148:151]\nMM v[152:155],a[112:115],a[68:71],v[152:155]\nTR a[50:51],v19 offset:1288\n"
"MM v[156:159],a[116:119],a[68:71],v[156:159]\nMM v[160:163],a[88:91],a[72:75],v[160:163]\nTR a[52:53],v19 offset:1536\n"
"MM v[164:167],a[92:95],a[72:75],v[164:167]\nMM v[168:171],a[96:99],a[72:75],v[168:171]\nTR a[54:55],v19 offset:1792\n"
"MM v[172:175],a[100:103],a[72:75],v[172:175]\nMM v[176:179],a[104:107],a[72:75],v[176:179]\nTR a[56:57],v19 offset:1544\n"
"MM v[180:183],a[108:111],a[72:75],v[180:183]\nMM v[184:187],a[112:115],a[72:75],v[184:187]\nTR a[58:59],v19 offset:1800\n"
"MM v[188:191],a[116:119],a[72:75],v[188:191]\nMM v[192:195],a[88:91],a[76:79],v[192:195]\nds_read_b128 a[4:7],v14 offset:2048\n"
"MM v[196:199],a[92:95],a[76:79],v[196:199]\nMM v[200:203],a[96:99],a[76:79],v[200:203]\nds_read_b128 a[8:11],v14 offset:4096\n"
"MM v[204:207],a[100:103],a[76:79],v[204:207]\nMM v[208:211],a[104:107],a[76:79],v[208:211]\nds_read_b128 a[12:15],v14 offset:6144\n"
"MM v[212:215],a[108:111],a[76:79],v[212:215]\nMM v[216:219],a[112:115],a[76:79],v[216:219]\nds_read_b128 a[16:19],v14 offset:8192\n"
"MM v[220:223],a[116:119],a[76:79],v[220:223]\nMM v[224:227],a[88:91],a[80:83],v[224:227]\nds_read_b128 a[20:23],v14 offset:10240\n"
"MM v[228:231],a[92:95],a[80:83],v[228:231]\nMM v[232:235],a[96:99],a[80:83],v[232:235]\nMM v[236:239],a[100:103],a[80:83],v[236:239]\n"
"MM v[240:243],a[104:107],a[80:83],v[240:243]\nMM v[244:247],a[108:111],a[80:83],v[244:247]\nMM v[248:251],a[112:115],a[80:83],v[248:251]\n"
"MM v[252:255],a[116:119],a[80:83],v[252:255]\ns_waitcnt vmcnt(8) lgkmcnt(0)\ns_barrier\nMM v[48:51],a[28:31],a[24:27],v[48:51]\n"
"ds_read_b128 a[84:87],v17 offset:0\nMM v[52:55],a[32:35],a[24:27],v[52:55]\ns_add_u32 m0,s63,32768\nMM v[56:59],a[36:39],a[24:27],v[56:59]\n"
"TR a[88:89],v18 offset:16384\nMM v[60:63],a[40:43],a[24:27],v[60:63]\nMM v[64:67],a[28:31],a[0:3],v[64:67]\nTR a[90:91],v18 offset:16640\n"
"MM v[68:71],a[32:35],a[0:3],v[68:71]\nMM v[72:75],a[36:39],a[0:3],v[72:75]\nTR a[92:93],v18 offset:16392\nMM v[76:79],a[40:43],a[0:3],v[76:79]\n"
"buffer_load_dwordx4 v11,s[52:55],s62 offen offset:0 nt lds\nMM v[80:83],a[44:47],a[0:3],v[80:83]\nTR a[94:95],v18 offset:16648\n"
"MM v[84:87],a[48:51],a[0:3],v[84:87]\nMM v[88:91],a[52:55],a[0:3],v[88:91]\nTR a[96:97],v18 offset:16896\nMM v[92:95],a[56:59],a[0:3],v[92:95]\n"
"MM v[96:99],a[28:31],a[4:7],v[96:99]\nTR a[98:99],v18 offset:17152\nMM v[100:103],a[32:35],a[4:7],v[100:103]\n"
"buffer_load_dwordx4 v0,s[52:55],s62 offen offset:1024 nt lds\nMM v[104:107],a[36:39],a[4:7],v[104:107]\nTR a[100:101],v18 offset:16904\n"
"MM v[108:111],a[40:43],a[4:7],v[108:111]\nMM v[112:115],a[44:47],a[4:7],v[112:115]\nTR a[102:103],v18 offset:17160\n"
"MM v[116:119],a[48:51],a[4:7],v[116:119]\nMM v[120:123],a[52:55],a[4:7],v[120:123]\nds_read_b128 a[60:63],v15 offset:0\n"
"MM v[124:127],a[56:59],a[4:7],v[124:127]\nbuffer_load_dwordx4 v11,s[52:55],s62 offen offset:2048 nt lds\nMM v[128:131],a[28:31],a[8:11],v[128:131]\n"
"TR a[104:105],v19 offset:17408\nMM v[132:135],a[32:35],a[8:11],v[132:135]\nMM v[136:139],a[36:39],a[8:11],v[136:139]\nTR a[106:107],v19 offset:17664\n"
"MM v[140:143],a[40:43],a[8:11],v[140:143]\nMM v[144:147],a[44:47],a[8:11],v[144:147]\nTR a[108:109],v19 offset:17416\n"
"MM v[148:151],a[48:51],a[8:11],v[148:151]\nbuffer_load_dwordx4 v0,s[52:55],s62 offen offset:3072 nt lds\nMM v[152:155],a[52:55],a[8:11],v[152:155]\n"
"TR a[110:111],v19 offset:17672\nMM v[156:159],a[56:59],a[8:11],v[156:159]\nMM v[160:163],a[28:31],a[12:15],v[160:163]\nTR a[112:113],v19 offset:17920\n"
"MM v[164:167],a[32:35],a[12:15],v[164:167]\nMM v[168:171],a[36:39],a[12:15],v[168:171]\nTR a[114:115],v19 offset:18176\n"
"MM v[172:175],a[40:43],a[12:15],v[172:175]\ns_add_u32 s62,s62,4096\nMM v[176:179],a[44:47],a[12:15],v[176:179]\nTR a[116:117],v19 offset:17928\n"
"MM v[180:183],a[48:51],a[12:15],v[180:183]\nMM v[184:187],a[52:55],a[12:15],v[184:187]\nTR a[118:119],v19 offset:18184\n"
"MM v[188:191],a[56:59],a[12:15],v[188:191]\nMM v[192:195],a[28:31],a[16:19],v[192:195]\nds_read_b128 a[64:67],v15 offset:2048\n"
"MM v[196:199],a[32:35],a[16:19],v[196:199]\ns_and_b32 s62,s62,0x3ffff\nMM v[200:203],a[36:39],a[16:19],v[200:203]\n"
"ds_read_b128 a[68:71],v15 offset:4096\nMM v[204:207],a[40:43],a[16:19],v[204:207]\nMM v[208:211],a[44:47],a[16:19],v[208:211]\n"
"ds_read_b128 a[72:75],v15 offset:6144\nMM v[212:215],a[48:51],a[16:19],v[212:215]\nMM v[216:219],a[52:55],a[16:19],v[216:219]\n"
"ds_read_b128 a[76:79],v15 offset:8192\nMM v[220:223],a[56:59],a[16:19],v[220:223]\nMM v[224:227],a[28:31],a[20:23],v[224:227]\n"
"ds_read_b128 a[80:83],v15 offset:10240\nMM v[228:231],a[32:35],a[20:23],v[228:231]\nMM v[232:235],a[36:39],a[20:23],v[232:235]\n"
"MM v[236:239],a[40:43],a[20:23],v[236:239]\nMM v[240:243],a[44:47],a[20:23],v[240:243]\nMM v[244:247],a[48:51],a[20:23],v[244:247]\n"
"MM v[248:251],a[52:55],a[20:23],v[248:251]\nMM v[252:255],a[56:59],a[20:23],v[252:255]\ns_waitcnt vmcnt(4) lgkmcnt(0)\ns_barrier\n"
"MM v[48:51],a[88:91],a[84:87],v[48:51]\nds_read_b128 a[128:131],v20 offset:0\nds_read_b128 a[132:135],v21 offset:0\n"
"MM v[52:55],a[92:95],a[84:87],v[52:55]\ns_add_u32 m0,s63,36864\nds_read_b128 a[136:139],v22 offset:0\nMM v[56:59],a[96:99],a[84:87],v[56:59]\n"
"ds_read_b128 a[140:143],v23 offset:0\nMM v[60:63],a[100:103],a[84:87],v[60:63]\nds_read_b128 a[0:3],v1 offset:0\n"
"MM v[64:67],a[88:91],a[60:63],v[64:67]\nds_read_b128 a[4:7],v2 offset:0\nMM v[68:71],a[92:95],a[60:63],v[68:71]\n"
"buffer_load_dwordx4 v11,s[52:55],s62 offen offset:0 nt lds\nMM v[72:75],a[96:99],a[60:63],v[72:75]\nds_read_b128 a[8:11],v3 offset:0\n"
"ds_read_b128 a[12:15],v4 offset:0\nMM v[76:79],a[100:103],a[60:63],v[76:79]\nds_read_b128 a[16:19],v1 offset:4096\n"
"ds_read_b128 a[20:23],v2 offset:4096\nMM v[80:83],a[104:107],a[60:63],v[80:83]\nds_read_b128 a[24:27],v3 offset:4096\nv_mul_f32 v48,s74,v48\n"
"MM v[84:87],a[108:111],a[60:63],v[84:87]\nbuffer_load_dwordx4 v0,s[52:55],s62 offen offset:1024 nt lds\nMM v[88:91],a[112:115],a[60:63],v[88:91]\n"
"ds_read_b128 a[28:31],v4 offset:4096\nv_mul_f32 v49,s74,v49\nMM v[92:95],a[116:119],a[60:63],v[92:95]\nv_mul_f32 v50,s74,v50\nv_mul_f32 v51,s74,v51\n"
"MM v[96:99],a[88:91],a[64:67],v[96:99]\nv_mul_f32 v52,s74,v52\nv_mul_f32 v53,s74,v53\nMM v[100:103],a[92:95],a[64:67],v[100:103]\n"
"buffer_load_dwordx4 v11,s[52:55],s62 offen offset:2048 nt lds\nMM v[104:107],a[96:99],a[64:67],v[104:107]\nv_mul_f32 v54,s74,v54\n"
"v_mul_f32 v55,s74,v55\nMM v[108:111],a[100:103],a[64:67],v[108:111]\nCV v48,v48,v49\nCV v49,v50,v51\nMM v[112:115],a[104:107],a[64:67],v[112:115]\n"
"CV v50,v52,v53\nCV v51,v54,v55\nMM v[116:119],a[108:111],a[64:67],v[116:119]\nbuffer_load_dwordx4 v0,s[52:55],s62 offen offset:3072 nt lds\n"
"MM v[120:123],a[112:115],a[64:67],v[120:123]\nds_write_b128 v36,v[48:51] offset:0\nv_accvgpr_write_b32 a240,v48\n"
"MM v[124:127],a[116:119],a[64:67],v[124:127]\nv_accvgpr_write_b32 a241,v49\nv_accvgpr_write_b32 a242,v50\nMM v[128:131],a[88:91],a[68:71],v[128:131]\n"
"v_accvgpr_write_b32 a243,v51\nv_mul_f32 v56,s74,v56\nMM v[132:135],a[92:95],a[68:71],v[132:135]\ns_add_u32 s62,s62,12288\nv_mul_f32 v57,s74,v57\n"
"MM v[136:139],a[96:99],a[68:71],v[136:139]\nv_mul_f32 v58,s74,v58\nv_mul_f32 v59,s74,v59\nMM v[140:143],a[100:103],a[68:71],v[140:143]\n"
"v_mul_f32 v60,s74,v60\nv_mul_f32 v61,s74,v61\nMM v[144:147],a[104:107],a[68:71],v[144:147]\nv_mul_f32 v62,s74,v62\nv_mul_f32 v63,s74,v63\n"
"MM v[148:151],a[108:111],a[68:71],v[148:151]\ns_and_b32 s62,s62,0x3ffff\nCV v56,v56,v57\nMM v[152:155],a[112:115],a[68:71],v[152:155]\nCV v57,v58,v59\n"
"CV v58,v60,v61\nMM v[156:159],a[116:119],a[68:71],v[156:159]\nCV v59,v62,v63\nds_write_b128 v36,v[56:59] offset:1024\n"
"MM v[160:163],a[88:91],a[72:75],v[160:163]\nv_accvgpr_write_b32 a244,v56\nv_accvgpr_write_b32 a245,v57\nMM v[164:167],a[92:95],a[72:75],v[164:167]\n"
"v_accvgpr_write_b32 a246,v58\nv_accvgpr_write_b32 a247,v59\nMM v[168:171],a[96:99],a[72:75],v[168:171]\nv_mul_f32 v64,s74,v64\nv_mul_f32 v65,s74,v65\n"
"MM v[172:175],a[100:103],a[72:75],v[172:175]\nv_mul_f32 v66,s74,v66\nv_mul_f32 v67,s74,v67\nMM v[176:179],a[104:107],a[72:75],v[176:179]\n"
"v_mul_f32 v68,s74,v68\nv_mul_f32 v69,s74,v69\nMM v[180:183],a[108:111],a[72:75],v[180:183]\nv_mul_f32 v70,s74,v70\nv_mul_f32 v71,s74,v71\n"
"MM v[184:187],a[112:115],a[72:75],v[184:187]\nCV v64,v64,v65\nCV v65,v66,v67\nMM v[188:191],a[116:119],a[72:75],v[188:191]\nCV v66,v68,v69\n"
"CV v67,v70,v71\nMM v[192:195],a[88:91],a[76:79],v[192:195]\nv_accvgpr_write_b32 a144,v64\nv_accvgpr_write_b32 a145,v65\n"
"MM v[196:199],a[92:95],a[76:79],v[196:199]\nv_accvgpr_write_b32 a146,v66\nv_accvgpr_write_b32 a147,v67\nMM v[200:203],a[96:99],a[76:79],v[200:203]\n"
"v_mul_f32 v72,s74,v72\nv_mul_f32 v73,s74,v73\nMM v[204:207],a[100:103],a[76:79],v[204:207]\nv_mul_f32 v74,s74,v74\nv_mul_f32 v75,s74,v75\n"
"MM v[208:211],a[104:107],a[76:79],v[208:211]\nv_mul_f32 v76,s74,v76\nv_mul_f32 v77,s74,v77\nMM v[212:215],a[108:111],a[76:79],v[212:215]\n"
"v_mul_f32 v78,s74,v78\nv_mul_f32 v79,s74,v79\nMM v[216:219],a[112:115],a[76:79],v[216:219]\nCV v72,v72,v73\nCV v73,v74,v75\n"
"MM v[220:223],a[116:119],a[76:79],v[220:223]\nCV v74,v76,v77\nCV v75,v78,v79\nMM v[224:227],a[88:91],a[80:83],v[224:227]\nv_accvgpr_write_b32 a148,v72\n"
"v_accvgpr_write_b32 a149,v73\nMM v[228:231],a[92:95],a[80:83],v[228:231]\nv_accvgpr_write_b32 a150,v74\nv_accvgpr_write_b32 a151,v75\n"
"MM v[232:235],a[96:99],a[80:83],v[232:235]\nMM v[236:239],a[100:103],a[80:83],v[236:239]\nMM v[240:243],a[104:107],a[80:83],v[240:243]\n"
"MM v[244:247],a[108:111],a[80:83],v[244:247]\nMM v[248:251],a[112:115],a[80:83],v[248:251]\nMM v[252:255],a[116:119],a[80:83],v[252:255]\n"
"v_pk_mul_f32 v[80:81],v[80:81],s[74:75]\nv_pk_mul_f32 v[82:83],v[82:83],s[74:75]\nv_pk_mul_f32 v[84:85],v[84:85],s[74:75]\n"
"v_pk_mul_f32 v[86:87],v[86:87],s[74:75]\nv_pk_mul_f32 v[88:89],v[88:89],s[74:75]\nv_pk_mul_f32 v[90:91],v[90:91],s[74:75]\n"
"v_pk_mul_f32 v[92:93],v[92:93],s[74:75]\nv_pk_mul_f32 v[94:95],v[94:95],s[74:75]\nv_pk_mul_f32 v[96:97],v[96:97],s[74:75]\n"
"v_pk_mul_f32 v[98:99],v[98:99],s[74:75]\nv_pk_mul_f32 v[100:101],v[100:101],s[74:75]\nv_pk_mul_f32 v[102:103],v[102:103],s[74:75]\n"
"v_pk_mul_f32 v[104:105],v[104:105],s[74:75]\nv_pk_mul_f32 v[106:107],v[106:107],s[74:75]\nv_pk_mul_f32 v[108:109],v[108:109],s[74:75]\n"
"v_pk_mul_f32 v[110:111],v[110:111],s[74:75]\nv_pk_mul_f32 v[112:113],v[112:113],s[74:75]\nv_pk_mul_f32 v[114:115],v[114:115],s[74:75]\n"
"v_pk_mul_f32 v[116:117],v[116:117],s[74:75]\nv_pk_mul_f32 v[118:119],v[118:119],s[74:75]\nv_pk_mul_f32 v[120:121],v[120:121],s[74:75]\n"
"v_pk_mul_f32 v[122:123],v[122:123],s[74:75]\nv_pk_mul_f32 v[124:125],v[124:125],s[74:75]\nv_pk_mul_f32 v[126:127],v[126:127],s[74:75]\n"
"v_pk_mul_f32 v[128:129],v[128:129],s[74:75]\nv_pk_mul_f32 v[130:131],v[130:131],s[74:75]\nv_pk_mul_f32 v[132:133],v[132:133],s[74:75]\n"
"v_pk_mul_f32 v[134:135],v[134:135],s[74:75]\nv_pk_mul_f32 v[136:137],v[136:137],s[74:75]\nv_pk_mul_f32 v[138:139],v[138:139],s[74:75]\n"
"v_pk_mul_f32 v[140:141],v[140:141],s[74:75]\nv_pk_mul_f32 v[142:143],v[142:143],s[74:75]\nv_pk_mul_f32 v[144:145],v[144:145],s[74:75]\n"
"v_pk_mul_f32 v[146:147],v[146:147],s[74:75]\nv_pk_mul_f32 v[148:149],v[148:149],s[74:75]\nv_pk_mul_f32 v[150:151],v[150:151],s[74:75]\n"
"v_pk_mul_f32 v[152:153],v[152:153],s[74:75]\nv_pk_mul_f32 v[154:155],v[154:155],s[74:75]\nv_pk_mul_f32 v[156:157],v[156:157],s[74:75]\n"
"v_pk_mul_f32 v[158:159],v[158:159],s[74:75]\nv_pk_mul_f32 v[160:161],v[160:161],s[74:75]\nv_pk_mul_f32 v[162:163],v[162:163],s[74:75]\n"
"v_pk_mul_f32 v[164:165],v[164:165],s[74:75]\nv_pk_mul_f32 v[166:167],v[166:167],s[74:75]\nv_pk_mul_f32 v[168:169],v[168:169],s[74:75]\n"
"v_pk_mul_f32 v[170:171],v[170:171],s[74:75]\nv_pk_mul_f32 v[172:173],v[172:173],s[74:75]\nv_pk_mul_f32 v[174:175],v[174:175],s[74:75]\n"
"v_pk_mul_f32 v[176:177],v[176:177],s[74:75]\nv_pk_mul_f32 v[178:179],v[178:179],s[74:75]\nv_pk_mul_f32 v[180:181],v[180:181],s[74:75]\n"
"v_pk_mul_f32 v[182:183],v[182:183],s[74:75]\nv_pk_mul_f32 v[184:185],v[184:185],s[74:75]\nv_pk_mul_f32 v[186:187],v[186:187],s[74:75]\n"
"v_pk_mul_f32 v[188:189],v[188:189],s[74:75]\nv_pk_mul_f32 v[190:191],v[190:191],s[74:75]\nv_pk_mul_f32 v[192:193],v[192:193],s[74:75]\n"
"v_pk_mul_f32 v[194:195],v[194:195],s[74:75]\nv_pk_mul_f32 v[196:197],v[196:197],s[74:75]\nv_pk_mul_f32 v[198:199],v[198:199],s[74:75]\n"
"v_pk_mul_f32 v[200:201],v[200:201],s[74:75]\nv_pk_mul_f32 v[202:203],v[202:203],s[74:75]\nv_pk_mul_f32 v[204:205],v[204:205],s[74:75]\n"
"v_pk_mul_f32 v[206:207],v[206:207],s[74:75]\nv_pk_mul_f32 v[208:209],v[208:209],s[74:75]\nv_pk_mul_f32 v[210:211],v[210:211],s[74:75]\n"
"v_pk_mul_f32 v[212:213],v[212:213],s[74:75]\nv_pk_mul_f32 v[214:215],v[214:215],s[74:75]\nv_pk_mul_f32 v[216:217],v[216:217],s[74:75]\n"
"v_pk_mul_f32 v[218:219],v[218:219],s[74:75]\nv_pk_mul_f32 v[220:221],v[220:221],s[74:75]\nv_pk_mul_f32 v[222:223],v[222:223],s[74:75]\n"
"v_pk_mul_f32 v[224:225],v[224:225],s[74:75]\nv_pk_mul_f32 v[226:227],v[226:227],s[74:75]\nv_pk_mul_f32 v[228:229],v[228:229],s[74:75]\n"
"v_pk_mul_f32 v[230:231],v[230:231],s[74:75]\nv_pk_mul_f32 v[232:233],v[232:233],s[74:75]\nv_pk_mul_f32 v[234:235],v[234:235],s[74:75]\n"
"v_pk_mul_f32 v[236:237],v[236:237],s[74:75]\nv_pk_mul_f32 v[238:239],v[238:239],s[74:75]\nv_pk_mul_f32 v[240:241],v[240:241],s[74:75]\n"
"v_pk_mul_f32 v[242:243],v[242:243],s[74:75]\nv_pk_mul_f32 v[244:245],v[244:245],s[74:75]\nv_pk_mul_f32 v[246:247],v[246:247],s[74:75]\n"
"v_pk_mul_f32 v[248:249],v[248:249],s[74:75]\nv_pk_mul_f32 v[250:251],v[250:251],s[74:75]\nv_pk_mul_f32 v[252:253],v[252:253],s[74:75]\n"
"v_pk_mul_f32 v[254:255],v[254:255],s[74:75]\ns_waitcnt lgkmcnt(0)\ns_memrealtime s[68:69]\nCV v80,v80,v81\nCV v81,v82,v83\ns_barrier\n"
"ds_read_b128 a[248:251],v37 offset:0\nds_read_b128 a[252:255],v37 offset:1024\nCV v82,v84,v85\nCV v83,v86,v87\n"
"v_mfma_f32_32x32x16_bf16 a[96:111],v[40:43],v[40:43],0\nv_accvgpr_write_b32 a152,v80\nv_accvgpr_write_b32 a153,v81\nv_accvgpr_write_b32 a154,v82\n"
"v_accvgpr_write_b32 a155,v83\nv_mfma_f32_32x32x16_bf16 a[112:127],v[40:43],v[40:43],0\nCV v88,v88,v89\nCV v89,v90,v91\nCV v90,v92,v93\nCV v91,v94,v95\n"
"MM v[24:27],a[128:131],a[240:243],0\nv_accvgpr_write_b32 a156,v88\nv_accvgpr_write_b32 a157,v89\ns_add_u32 m0,s63,65536\n"
"MM v[24:27],a[132:135],a[244:247],v[24:27]\nv_accvgpr_write_b32 a158,v90\nv_accvgpr_write_b32 a159,v91\ns_waitcnt lgkmcnt(1)\n"
"MM v[24:27],a[136:139],a[248:251],v[24:27]\nCV v96,v96,v97\nCV v97,v98,v99\ns_waitcnt lgkmcnt(0)\nMM v[24:27],a[140:143],a[252:255],v[24:27]\n"
"CV v98,v100,v101\nCV v99,v102,v103\nv_mfma_f32_32x32x16_bf16 a[64:79],v[40:43],v[40:43],0\nv_accvgpr_write_b32 a160,v96\nv_accvgpr_write_b32 a161,v97\n"
"v_accvgpr_write_b32 a162,v98\nv_accvgpr_write_b32 a163,v99\nv_mfma_f32_32x32x16_bf16 a[80:95],v[40:43],v[40:43],0\nCV v104,v104,v105\n"
"CV v105,v106,v107\nCV v106,v108,v109\nCV v107,v110,v111\nv_max3_f32 v24,v24,v25,v26\nv_accvgpr_write_b32 a164,v104\n"
"buffer_load_dwordx4 v7,s[48:51],s61 offen offset:0 nt lds\nv_accvgpr_write_b32 a165,v105\nv_min_f32_e64 v60,-v24,-v27\nv_accvgpr_write_b32 a166,v106\n"
"v_accvgpr_write_b32 a167,v107\nv_min_f32_e64 v61,-v24,-v27\nCV v112,v112,v113\nCV v113,v114,v115\nv_min_f32_e64 v62,-v24,-v27\nCV v114,v116,v117\n"
"CV v115,v118,v119\nv_min_f32_e64 v63,-v24,-v27\nv_accvgpr_write_b32 a168,v112\nv_accvgpr_write_b32 a169,v113\nv_accvgpr_write_b32 a170,v114\n"
"v_accvgpr_write_b32 a171,v115\nMM v[24:27],a[128:131],a[144:147],0\nCV v120,v120,v121\nCV v121,v122,v123\nMM v[24:27],a[132:135],a[148:151],v[24:27]\n"
"CV v122,v124,v125\nCV v123,v126,v127\nbuffer_load_dwordx4 v8,s[48:51],s61 offen offset:1024 nt lds\nMM v[24:27],a[136:139],a[152:155],v[24:27]\n"
"v_accvgpr_write_b32 a172,v120\nv_accvgpr_write_b32 a173,v121\nMM v[24:27],a[140:143],a[156:159],v[24:27]\nv_accvgpr_write_b32 a174,v122\n"
"v_accvgpr_write_b32 a175,v123\nv_mfma_f32_32x32x16_bf16 v[64:79],v[40:43],v[40:43],0\nCV v128,v128,v129\nCV v129,v130,v131\nCV v130,v132,v133\n"
"CV v131,v134,v135\nv_mfma_f32_32x32x16_bf16 v[80:95],v[40:43],v[40:43],0\nv_accvgpr_write_b32 a176,v128\nv_accvgpr_write_b32 a177,v129\n"
"v_accvgpr_write_b32 a178,v130\nv_accvgpr_write_b32 a179,v131\nv_max3_f32 v24,v24,v25,v26\nCV v136,v136,v137\nCV v137,v138,v139\n"
"v_min_f32_e64 v12,-v24,-v27\nCV v138,v140,v141\nbuffer_load_dwordx4 v9,s[48:51],s61 offen offset:2048 nt lds\nCV v139,v142,v143\n"
"v_accvgpr_write_b32 a180,v136\nv_accvgpr_write_b32 a181,v137\nMM v[28:31],a[128:131],a[160:163],0\nv_accvgpr_write_b32 a182,v138\n"
"v_accvgpr_write_b32 a183,v139\nMM v[28:31],a[132:135],a[164:167],v[28:31]\nCV v144,v144,v145\nCV v145,v146,v147\n"
"MM v[28:31],a[136:139],a[168:171],v[28:31]\nCV v146,v148,v149\nCV v147,v150,v151\nMM v[28:31],a[140:143],a[172:175],v[28:31]\n"
"v_accvgpr_write_b32 a184,v144\nv_accvgpr_write_b32 a185,v145\nv_mfma_f32_32x32x16_bf16 v[96:111],v[40:43],v[40:43],0\nv_accvgpr_write_b32 a186,v146\n"
"v_accvgpr_write_b32 a187,v147\nCV v152,v152,v153\nCV v153,v154,v155\nv_mfma_f32_32x32x16_bf16 v[112:127],v[40:43],v[40:43],0\n"
"buffer_load_dwordx4 v10,s[48:51],s61 offen offset:3072 nt lds\nCV v154,v156,v157\nCV v155,v158,v159\nv_accvgpr_write_b32 a188,v152\n"
"v_accvgpr_write_b32 a189,v153\nv_max3_f32 v28,v28,v29,v30\nv_accvgpr_write_b32 a190,v154\nv_accvgpr_write_b32 a191,v155\nv_min_f32_e64 v13,-v28,-v31\n"
"CV v160,v160,v161\nCV v161,v162,v163\nCV v162,v164,v165\nCV v163,v166,v167\nMM v[32:35],a[128:131],a[176:179],0\nv_accvgpr_write_b32 a192,v160\n"
"v_accvgpr_write_b32 a193,v161\nMM v[32:35],a[132:135],a[180:183],v[32:35]\nv_accvgpr_write_b32 a194,v162\nv_accvgpr_write_b32 a195,v163\n"
"MM v[32:35],a[136:139],a[184:187],v[32:35]\nCV v168,v168,v169\nCV v169,v170,v171\ns_add_u32 s61,s61,4096\nMM v[32:35],a[140:143],a[188:191],v[32:35]\n"
"CV v170,v172,v173\nCV v171,v174,v175\nv_mfma_f32_32x32x16_bf16 v[128:143],v[40:43],v[40:43],0\nv_accvgpr_write_b32 a196,v168\n"
"v_accvgpr_write_b32 a197,v169\nv_accvgpr_write_b32 a198,v170\nv_accvgpr_write_b32 a199,v171\nv_mfma_f32_32x32x16_bf16 v[144:159],v[40:43],v[40:43],0\n"
"CV v176,v176,v177\nCV v177,v178,v179\ns_waitcnt vmcnt(4)\nCV v178,v180,v181\nCV v179,v182,v183\nv_max3_f32 v32,v32,v33,v34\n"
"v_accvgpr_write_b32 a200,v176\nv_accvgpr_write_b32 a201,v177\nv_min_f32_e64 v44,-v32,-v35\nv_accvgpr_write_b32 a202,v178\n"
"v_accvgpr_write_b32 a203,v179\nv_min_f32_e64 v45,-v32,-v35\ns_and_b32 s61,s61,0x3ffff\nCV v184,v184,v185\nCV v185,v186,v187\n"
"v_min_f32_e64 v46,-v32,-v35\nCV v186,v188,v189\nCV v187,v190,v191\nv_min_f32_e64 v47,-v32,-v35\nv_accvgpr_write_b32 a204,v184\n"
"v_accvgpr_write_b32 a205,v185\nv_accvgpr_write_b32 a206,v186\nv_accvgpr_write_b32 a207,v187\nCV v192,v192,v193\nCV v193,v194,v195\n"
"TR a[32:33],v5 offset:0\nCV v194,v196,v197\nCV v195,v198,v199\nMM v[24:27],a[128:131],a[192:195],0\nv_accvgpr_write_b32 a208,v192\n"
"v_accvgpr_write_b32 a209,v193\nMM v[24:27],a[132:135],a[196:199],v[24:27]\nv_accvgpr_write_b32 a210,v194\nv_accvgpr_write_b32 a211,v195\n"
"s_add_u32 m0,s63,69632\nMM v[24:27],a[136:139],a[200:203],v[24:27]\nCV v200,v200,v201\nCV v201,v202,v203\nMM v[24:27],a[140:143],a[204:207],v[24:27]\n"
"CV v202,v204,v205\nCV v203,v206,v207\nv_mfma_f32_32x32x16_bf16 v[160:175],v[40:43],v[40:43],0\nv_accvgpr_write_b32 a212,v200\n"
"v_accvgpr_write_b32 a213,v201\nTR a[34:35],v5 offset:4096\nv_accvgpr_write_b32 a214,v202\nv_accvgpr_write_b32 a215,v203\n"
"v_mfma_f32_32x32x16_bf16 v[176:191],v[40:43],v[40:43],0\nCV v208,v208,v209\nCV v209,v210,v211\nTR a[36:37],v6 offset:0\nCV v210,v212,v213\n"
"CV v211,v214,v215\nv_max3_f32 v24,v24,v25,v26\nv_accvgpr_write_b32 a216,v208\nv_accvgpr_write_b32 a217,v209\n"
"buffer_load_dwordx4 v7,s[48:51],s61 offen offset:0 nt lds\nv_min_f32_e64 v48,-v24,-v27\nv_accvgpr_write_b32 a218,v210\nv_accvgpr_write_b32 a219,v211\n"
"v_min_f32_e64 v49,-v24,-v27\nCV v216,v216,v217\nCV v217,v218,v219\nv_min_f32_e64 v50,-v24,-v27\nCV v218,v220,v221\nCV v219,v222,v223\n"
"v_min_f32_e64 v51,-v24,-v27\nv_accvgpr_write_b32 a220,v216\nv_accvgpr_write_b32 a221,v217\nv_accvgpr_write_b32 a222,v218\n"
"v_accvgpr_write_b32 a223,v219\nCV v224,v224,v225\nCV v225,v226,v227\nTR a[38:39],v6 offset:4096\nCV v226,v228,v229\nCV v227,v230,v231\n"
"MM v[28:31],a[128:131],a[208:211],0\nv_accvgpr_write_b32 a224,v224\nbuffer_load_dwordx4 v8,s[48:51],s61 offen offset:1024 nt lds\n"
"v_accvgpr_write_b32 a225,v225\nMM v[28:31],a[132:135],a[212:215],v[28:31]\nv_accvgpr_write_b32 a226,v226\nv_accvgpr_write_b32 a227,v227\n"
"MM v[28:31],a[136:139],a[216:219],v[28:31]\nCV v232,v232,v233\nCV v233,v234,v235\nMM v[28:31],a[140:143],a[220:223],v[28:31]\nCV v234,v236,v237\n"
"CV v235,v238,v239\nv_mfma_f32_32x32x16_bf16 v[192:207],v[40:43],v[40:43],0\nv_accvgpr_write_b32 a228,v232\nv_accvgpr_write_b32 a229,v233\n"
"TR a[40:41],v5 offset:512\nv_accvgpr_write_b32 a230,v234\nv_accvgpr_write_b32 a231,v235\nv_mfma_f32_32x32x16_bf16 v[208:223],v[40:43],v[40:43],0\n"
"CV v240,v240,v241\nCV v241,v242,v243\nTR a[42:43],v5 offset:4608\nCV v242,v244,v245\nbuffer_load_dwordx4 v9,s[48:51],s61 offen offset:2048 nt lds\n"
"CV v243,v246,v247\nv_max3_f32 v28,v28,v29,v30\nv_accvgpr_write_b32 a232,v240\nv_accvgpr_write_b32 a233,v241\nv_min_f32_e64 v52,-v28,-v31\n"
"v_accvgpr_write_b32 a234,v242\nv_accvgpr_write_b32 a235,v243\nv_min_f32_e64 v53,-v28,-v31\nCV v248,v248,v249\nCV v249,v250,v251\n"
"v_min_f32_e64 v54,-v28,-v31\nCV v250,v252,v253\nCV v251,v254,v255\nv_min_f32_e64 v55,-v28,-v31\nv_accvgpr_write_b32 a236,v248\n"
"v_accvgpr_write_b32 a237,v249\nv_accvgpr_write_b32 a238,v250\nv_accvgpr_write_b32 a239,v251\nTR a[44:45],v6 offset:512\n"
"MM v[32:35],a[128:131],a[224:227],0\nMM v[32:35],a[132:135],a[228:231],v[32:35]\nbuffer_load_dwordx4 v10,s[48:51],s61 offen offset:3072 nt lds\n"
"MM v[32:35],a[136:139],a[232:235],v[32:35]\nMM v[32:35],a[140:143],a[236:239],v[32:35]\nv_mfma_f32_32x32x16_bf16 v[224:239],v[40:43],v[40:43],0\n"
"TR a[46:47],v6 offset:4608\nv_mfma_f32_32x32x16_bf16 v[240:255],v[40:43],v[40:43],0\nTR a[48:49],v5 offset:1024\ns_nop 3\nv_max3_f32 v32,v32,v33,v34\n"
"v_min_f32_e64 v56,-v32,-v35\nv_min_f32_e64 v57,-v32,-v35\nv_min_f32_e64 v58,-v32,-v35\nv_min_f32_e64 v59,-v32,-v35\nTR a[50:51],v5 offset:5120\n"
"TR a[52:53],v6 offset:1024\nTR a[54:55],v6 offset:5120\nTR a[56:57],v5 offset:1536\nTR a[58:59],v5 offset:5632\nTR a[60:61],v6 offset:1536\n"
"TR a[62:63],v6 offset:5632\nv_mbcnt_lo_u32_b32 v15,-1,0\nv_mbcnt_hi_u32_b32 v15,-1,v15\nv_and_b32 v15,3,v15\ns_add_u32 s61,s61,12288\n"
"v_cmp_eq_u32 vcc,0,v15\nv_mov_b32 v14,0x3f803f80\ns_nop 0\nv_cndmask_b32 v14,0,v14,vcc\nv_accvgpr_write_b32 a124,v14\nv_accvgpr_write_b32 a125,v14\n"
"v_accvgpr_write_b32 a126,v14\nv_accvgpr_write_b32 a127,v14\ns_add_u32 s64,s63,0\ns_add_u32 s65,s63,65536\ns_add_u32 s66,s63,98304\ns_mov_b32 s90,8\n"
"v_mov_b32 v36,v12\nv_mov_b32 v37,v12\nv_mov_b32 v38,v12\nv_mov_b32 v39,v12\ns_mov_b64 s[40:41],%[wo]\ns_add_u32 s40,s40,%[owt]\ns_addc_u32 s41,s41,0\n"
"s_and_b32 s41,s41,0xffff\ns_mov_b32 s42,6144\ns_mov_b32 s73,0\ns_cmp_gt_u32 s93,100\ns_cselect_b32 s89,46,0\ns_cselect_b32 s85,136,0\n"
"s_lshr_b32 s72,s89,3\ns_mov_b32 s71,s93\ns_sub_i32 s88,s93,315\ns_mov_b32 s93,0\ns_waitcnt lgkmcnt(0)\ns_waitcnt lgkmcnt(0)\ns_add_u32 s70,s68,s71\n"
"s_sub_u32 s70,s70,41\nv_mov_b32 v40,v13\nv_mov_b32 v41,v13\nv_mov_b32 v42,v13\nv_mov_b32 v43,v13\ns_and_b32 s61,s61,0x3ffff\n"
"MM v[12:15],a[0:3],a[240:243],v[60:63]\nMM v[12:15],a[4:7],a[244:247],v[12:15]\nMM v[12:15],a[8:11],a[248:251],v[12:15]\n"
"MM v[12:15],a[12:15],a[252:255],v[12:15]\nMM v[16:19],a[28:31],a[252:255],v[60:63]\nMM v[16:19],a[24:27],a[248:251],v[16:19]\n"
"MM v[16:19],a[20:23],a[244:247],v[16:19]\nMM v[16:19],a[16:19],a[240:243],v[16:19]\nMM v[20:23],a[0:3],a[144:147],v[36:39]\ns_nop 2\nv_exp_f32 v12,v12\n"
"MM v[20:23],a[4:7],a[148:151],v[20:23]\nv_exp_f32 v13,v13\nMM v[20:23],a[8:11],a[152:155],v[20:23]\nv_exp_f32 v14,v14\nCV v28,v12,v13\n"
"MM v[20:23],a[12:15],a[156:159],v[20:23]\nv_exp_f32 v15,v15\nMM v[24:27],a[28:31],a[156:159],v[36:39]\nv_exp_f32 v16,v16\nCV v29,v14,v15\n"
"MM v[24:27],a[24:27],a[152:155],v[24:27]\nv_exp_f32 v17,v17\nMM v[24:27],a[20:23],a[148:151],v[24:27]\nv_exp_f32 v18,v18\nCV v30,v16,v17\n"
"MM v[24:27],a[16:19],a[144:147],v[24:27]\nv_exp_f32 v19,v19\ns_nop 0\nCV v31,v18,v19\nMM v[12:15],a[0:3],a[160:163],v[40:43]\nv_exp_f32 v20,v20\n"
"MM v[12:15],a[4:7],a[164:167],v[12:15]\nMM v[12:15],a[8:11],a[168:171],v[12:15]\nv_exp_f32 v21,v21\nMM v[12:15],a[12:15],a[172:175],v[12:15]\n"
"CV v32,v20,v21\nMM v[16:19],a[28:31],a[172:175],v[40:43]\nv_exp_f32 v22,v22\nMM v[16:19],a[24:27],a[168:171],v[16:19]\n"
"MM v[16:19],a[20:23],a[164:167],v[16:19]\nv_exp_f32 v23,v23\nMM v[16:19],a[16:19],a[160:163],v[16:19]\nCV v33,v22,v23\n"
"MM a[64:67],a[32:35],v[28:31],a[64:67]\nv_exp_f32 v24,v24\nMM a[68:71],a[36:39],v[28:31],a[68:71]\nMM a[72:75],a[40:43],v[28:31],a[72:75]\n"
"v_exp_f32 v25,v25\nMM a[76:79],a[44:47],v[28:31],a[76:79]\nCV v34,v24,v25\nMM a[80:83],a[48:51],v[28:31],a[80:83]\nv_exp_f32 v26,v26\n"
"MM a[84:87],a[52:55],v[28:31],a[84:87]\nMM a[88:91],a[56:59],v[28:31],a[88:91]\nv_exp_f32 v27,v27\nMM a[92:95],a[60:63],v[28:31],a[92:95]\n"
"CV v35,v26,v27\nMM a[120:123],a[124:127],v[28:31],a[120:123]\ns_branch L_mid_%=\nL_sl0_0_%=:\ns_nop 15\ns_nop 3\ns_cmp_gt_u32 s72,8\n"
"s_cbranch_scc0 L_sl0_0b_%=\ns_nop 15\ns_nop 15\ns_branch L_sl0_0b_%=\nL_sl0_1_%=:\ns_nop 15\ns_nop 3\ns_cmp_gt_u32 s72,9\ns_cbranch_scc0 L_sl0_1b_%=\n"
"s_nop 15\ns_nop 15\ns_branch L_sl0_1b_%=\nL_sl0_2_%=:\ns_nop 15\ns_nop 3\ns_cmp_gt_u32 s72,10\ns_cbranch_scc0 L_sl0_2b_%=\ns_nop 15\ns_nop 15\n"
"s_branch L_sl0_2b_%=\nL_sl0_3_%=:\ns_nop 15\ns_nop 3\ns_cmp_gt_u32 s72,11\ns_cbranch_scc0 L_sl0_3b_%=\ns_nop 15\ns_nop 15\ns_branch L_sl0_3b_%=\n"
"L_sl0_4_%=:\ns_nop 15\ns_nop 3\ns_cmp_gt_u32 s72,12\ns_cbranch_scc0 L_sl0_4b_%=\ns_nop 15\ns_nop 15\ns_branch L_sl0_4b_%=\nL_sl0_5_%=:\ns_nop 15\n"
"s_nop 3\ns_cmp_gt_u32 s72,13\ns_cbranch_scc0 L_sl0_5b_%=\ns_nop 15\ns_nop 15\ns_branch L_sl0_5b_%=\nL_sl0_6_%=:\ns_nop 15\ns_nop 3\n"
"s_cmp_gt_u32 s72,14\ns_cbranch_scc0 L_sl0_6b_%=\ns_nop 15\ns_nop 15\ns_branch L_sl0_6b_%=\nL_sl0_7_%=:\ns_nop 15\ns_nop 3\ns_cmp_gt_u32 s72,15\n"
"s_cbranch_scc0 L_sl0_7b_%=\ns_nop 15\ns_nop 15\ns_branch L_sl0_7b_%=\nL_sl1_0_%=:\ns_nop 15\ns_nop 3\ns_cmp_gt_u32 s72,8\ns_cbranch_scc0 L_sl1_0b_%=\n"
"s_nop 15\ns_nop 15\ns_branch L_sl1_0b_%=\nL_sl1_1_%=:\ns_nop 15\ns_nop 3\ns_cmp_gt_u32 s72,9\ns_cbranch_scc0 L_sl1_1b_%=\ns_nop 15\ns_nop 15\n"
"s_branch L_sl1_1b_%=\nL_sl1_2_%=:\ns_nop 15\ns_nop 3\ns_cmp_gt_u32 s72,10\ns_cbranch_scc0 L_sl1_2b_%=\ns_nop 15\ns_nop 15\ns_branch L_sl1_2b_%=\n"
"L_sl1_3_%=:\ns_nop 15\ns_nop 3\ns_cmp_gt_u32 s72,11\ns_cbranch_scc0 L_sl1_3b_%=\ns_nop 15\ns_nop 15\ns_branch L_sl1_3b_%=\nL_sl1_4_%=:\ns_nop 15\n"
"s_nop 3\ns_cmp_gt_u32 s72,12\ns_cbranch_scc0 L_sl1_4b_%=\ns_nop 15\ns_nop 15\ns_branch L_sl1_4b_%=\nL_sl1_5_%=:\ns_nop 15\ns_nop 3\n"
"s_cmp_gt_u32 s72,13\ns_cbranch_scc0 L_sl1_5b_%=\ns_nop 15\ns_nop 15\ns_branch L_sl1_5b_%=\nL_sl1_6_%=:\ns_nop 15\ns_nop 3\ns_cmp_gt_u32 s72,14\n"
"s_cbranch_scc0 L_sl1_6b_%=\ns_nop 15\ns_nop 15\ns_branch L_sl1_6b_%=\nL_sl1_7_%=:\ns_nop 15\ns_nop 3\ns_cmp_gt_u32 s72,15\ns_cbranch_scc0 L_sl1_7b_%=\n"
"s_nop 15\ns_nop 15\ns_branch L_sl1_7b_%=\n.p2align 6\nL_top_%=:\nMM v[20:23],a[0:3],a[176:179],v[44:47]\nv_exp_f32 v12,v12\n"
"MM v[20:23],a[4:7],a[180:183],v[20:23]\ns_cmp_gt_u32 s72,6\ns_cbranch_scc1 L_sl0_6_%=\nL_sl0_6b_%=:\nMM v[20:23],a[8:11],a[184:187],v[20:23]\n"
"v_exp_f32 v13,v13\nMM v[20:23],a[12:15],a[188:191],v[20:23]\nCV v28,v12,v13\ns_add_u32 m0,s66,0\nMM v[24:27],a[28:31],a[188:191],v[44:47]\n"
"v_exp_f32 v14,v14\nMM v[24:27],a[24:27],a[184:187],v[24:27]\nMM v[24:27],a[20:23],a[180:183],v[24:27]\nv_exp_f32 v15,v15\n"
"MM v[24:27],a[16:19],a[176:179],v[24:27]\nCV v29,v14,v15\ns_waitcnt lgkmcnt(0)\nMM v[64:67],a[32:35],v[32:35],v[64:67]\nv_exp_f32 v16,v16\n"
"MM v[68:71],a[36:39],v[32:35],v[68:71]\nbuffer_load_dwordx4 v7,s[48:51],s61 offen offset:0 nt lds\nMM v[72:75],a[40:43],v[32:35],v[72:75]\n"
"v_exp_f32 v17,v17\nMM v[76:79],a[44:47],v[32:35],v[76:79]\nCV v30,v16,v17\nMM v[80:83],a[48:51],v[32:35],v[80:83]\nv_exp_f32 v18,v18\n"
"MM v[84:87],a[52:55],v[32:35],v[84:87]\ns_xor_b32 s91,s64,s65\ns_or_b32 s91,s91,8192\nMM v[88:91],a[56:59],v[32:35],v[88:91]\nv_exp_f32 v19,v19\n"
"MM v[92:95],a[60:63],v[32:35],v[92:95]\nCV v31,v18,v19\nv_xor_b32 v1,s91,v1\nMM a[96:99],a[124:127],v[32:35],a[96:99]\nv_xor_b32 v2,s91,v2\n"
"v_xor_b32 v3,s91,v3\nMM v[12:15],a[0:3],a[192:195],v[48:51]\nv_exp_f32 v20,v20\nMM v[12:15],a[4:7],a[196:199],v[12:15]\nv_xor_b32 v4,s91,v4\n"
"MM v[12:15],a[8:11],a[200:203],v[12:15]\nv_exp_f32 v21,v21\nMM v[12:15],a[12:15],a[204:207],v[12:15]\nCV v32,v20,v21\n"
"MM v[16:19],a[28:31],a[204:207],v[48:51]\nv_exp_f32 v22,v22\nMM v[16:19],a[24:27],a[200:203],v[16:19]\ns_cmp_gt_u32 s72,0\ns_cbranch_scc1 L_sl0_0_%=\n"
"L_sl0_0b_%=:\nMM v[16:19],a[20:23],a[196:199],v[16:19]\nv_exp_f32 v23,v23\nMM v[16:19],a[16:19],a[192:195],v[16:19]\nCV v33,v22,v23\n"
"MM v[96:99],a[32:35],v[28:31],v[96:99]\nv_exp_f32 v24,v24\nMM v[100:103],a[36:39],v[28:31],v[100:103]\n"
"buffer_load_dwordx4 v8,s[48:51],s61 offen offset:1024 nt lds\nMM v[104:107],a[40:43],v[28:31],v[104:107]\nv_exp_f32 v25,v25\n"
"MM v[108:111],a[44:47],v[28:31],v[108:111]\nCV v34,v24,v25\nMM v[112:115],a[48:51],v[28:31],v[112:115]\nv_exp_f32 v26,v26\n"
"MM v[116:119],a[52:55],v[28:31],v[116:119]\nbuffer_load_dwordx4 v9,s[48:51],s61 offen offset:2048 nt lds\nMM v[120:123],a[56:59],v[28:31],v[120:123]\n"
"v_exp_f32 v27,v27\nMM v[124:127],a[60:63],v[28:31],v[124:127]\nCV v35,v26,v27\nMM a[100:103],a[124:127],v[28:31],a[100:103]\n"
"buffer_load_dwordx4 v10,s[48:51],s61 offen offset:3072 nt lds\nMM v[20:23],a[0:3],a[208:211],v[52:55]\nv_exp_f32 v12,v12\n"
"MM v[20:23],a[4:7],a[212:215],v[20:23]\ns_cmp_gt_u32 s72,7\ns_cbranch_scc1 L_sl0_7_%=\nL_sl0_7b_%=:\nMM v[20:23],a[8:11],a[216:219],v[20:23]\n"
"v_exp_f32 v13,v13\nMM v[20:23],a[12:15],a[220:223],v[20:23]\nCV v28,v12,v13\ns_add_u32 s61,s61,4096\nMM v[24:27],a[28:31],a[220:223],v[52:55]\n"
"v_exp_f32 v14,v14\nMM v[24:27],a[24:27],a[216:219],v[24:27]\ns_and_b32 s61,s61,0x3ffff\nMM v[24:27],a[20:23],a[212:215],v[24:27]\nv_exp_f32 v15,v15\n"
"MM v[24:27],a[16:19],a[208:211],v[24:27]\nCV v29,v14,v15\nMM v[128:131],a[32:35],v[32:35],v[128:131]\nv_exp_f32 v16,v16\n"
"MM v[132:135],a[36:39],v[32:35],v[132:135]\nMM v[136:139],a[40:43],v[32:35],v[136:139]\nv_exp_f32 v17,v17\nMM v[140:143],a[44:47],v[32:35],v[140:143]\n"
"CV v30,v16,v17\nMM v[144:147],a[48:51],v[32:35],v[144:147]\nv_exp_f32 v18,v18\nMM v[148:151],a[52:55],v[32:35],v[148:151]\ns_add_u32 m0,s66,4096\n"
"MM v[152:155],a[56:59],v[32:35],v[152:155]\nv_exp_f32 v19,v19\nMM v[156:159],a[60:63],v[32:35],v[156:159]\nCV v31,v18,v19\n"
"MM a[104:107],a[124:127],v[32:35],a[104:107]\ns_waitcnt vmcnt(12)\nMM v[12:15],a[0:3],a[224:227],v[56:59]\nv_exp_f32 v20,v20\n"
"MM v[12:15],a[4:7],a[228:231],v[12:15]\nds_read_b128 a[0:3],v1 offset:0\nds_read_b128 a[4:7],v2 offset:0\nMM v[12:15],a[8:11],a[232:235],v[12:15]\n"
"v_exp_f32 v21,v21\nMM v[12:15],a[12:15],a[236:239],v[12:15]\nCV v32,v20,v21\nds_read_b128 a[8:11],v3 offset:0\n"
"MM v[16:19],a[28:31],a[236:239],v[56:59]\nv_exp_f32 v22,v22\nMM v[16:19],a[24:27],a[232:235],v[16:19]\nds_read_b128 a[12:15],v4 offset:0\n"
"ds_read_b128 a[28:31],v4 offset:4096\nMM v[16:19],a[20:23],a[228:231],v[16:19]\nv_exp_f32 v23,v23\nMM v[16:19],a[16:19],a[224:227],v[16:19]\n"
"CV v33,v22,v23\nds_read_b128 a[24:27],v3 offset:4096\nMM v[160:163],a[32:35],v[28:31],v[160:163]\nv_exp_f32 v24,v24\n"
"MM v[164:167],a[36:39],v[28:31],v[164:167]\nds_read_b128 a[20:23],v2 offset:4096\nds_read_b128 a[16:19],v1 offset:4096\n"
"MM v[168:171],a[40:43],v[28:31],v[168:171]\nv_exp_f32 v25,v25\nMM v[172:175],a[44:47],v[28:31],v[172:175]\nCV v34,v24,v25\n"
"MM v[176:179],a[48:51],v[28:31],v[176:179]\nv_exp_f32 v26,v26\nMM v[180:183],a[52:55],v[28:31],v[180:183]\ns_cmp_gt_u32 s72,2\n"
"s_cbranch_scc1 L_sl0_2_%=\nL_sl0_2b_%=:\nMM v[184:187],a[56:59],v[28:31],v[184:187]\nv_exp_f32 v27,v27\nMM v[188:191],a[60:63],v[28:31],v[188:191]\n"
"CV v35,v26,v27\nMM a[108:111],a[124:127],v[28:31],a[108:111]\nbuffer_load_dwordx4 v7,s[48:51],s61 offen offset:0 nt lds\ns_waitcnt lgkmcnt(0)\n"
"MM v[20:23],a[0:3],a[240:243],v[60:63]\nv_exp_f32 v12,v12\nMM v[20:23],a[4:7],a[244:247],v[20:23]\n"
"buffer_load_dwordx4 v8,s[48:51],s61 offen offset:1024 nt lds\nMM v[20:23],a[8:11],a[248:251],v[20:23]\nv_exp_f32 v13,v13\n"
"MM v[20:23],a[12:15],a[252:255],v[20:23]\nCV v28,v12,v13\nMM v[24:27],a[28:31],a[252:255],v[60:63]\nv_exp_f32 v14,v14\n"
"MM v[24:27],a[24:27],a[248:251],v[24:27]\nbuffer_load_dwordx4 v9,s[48:51],s61 offen offset:2048 nt lds\nMM v[24:27],a[20:23],a[244:247],v[24:27]\n"
"v_exp_f32 v15,v15\nMM v[24:27],a[16:19],a[240:243],v[24:27]\nCV v29,v14,v15\nbuffer_load_dwordx4 v10,s[48:51],s61 offen offset:3072 nt lds\n"
"MM v[192:195],a[32:35],v[32:35],v[192:195]\nv_exp_f32 v16,v16\nMM v[196:199],a[36:39],v[32:35],v[196:199]\nv_exp_f32 v20,v20\n"
"MM v[200:203],a[40:43],v[32:35],v[200:203]\nv_exp_f32 v17,v17\nMM v[204:207],a[44:47],v[32:35],v[204:207]\nCV v30,v16,v17\nv_xor_b32 v5,172032,v5\n"
"MM v[208:211],a[48:51],v[32:35],v[208:211]\nv_exp_f32 v18,v18\nMM v[212:215],a[52:55],v[32:35],v[212:215]\nv_exp_f32 v21,v21\n"
"MM v[216:219],a[56:59],v[32:35],v[216:219]\nv_exp_f32 v19,v19\nMM v[220:223],a[60:63],v[32:35],v[220:223]\nCV v31,v18,v19\nv_xor_b32 v6,172032,v6\n"
"MM a[112:115],a[124:127],v[32:35],a[112:115]\nv_exp_f32 v22,v22\nMM v[12:15],a[0:3],a[144:147],v[36:39]\nv_exp_f32 v23,v23\n"
"MM v[12:15],a[4:7],a[148:151],v[12:15]\nCV v32,v20,v21\nCV v33,v22,v23\nMM v[12:15],a[8:11],a[152:155],v[12:15]\nv_exp_f32 v24,v24\n"
"MM v[12:15],a[12:15],a[156:159],v[12:15]\nv_exp_f32 v25,v25\nMM v[16:19],a[28:31],a[156:159],v[36:39]\nv_exp_f32 v26,v26\n"
"MM v[16:19],a[24:27],a[152:155],v[16:19]\nv_exp_f32 v27,v27\nMM v[16:19],a[20:23],a[148:151],v[16:19]\nCV v34,v24,v25\nCV v35,v26,v27\n"
"MM v[16:19],a[16:19],a[144:147],v[16:19]\ns_waitcnt vmcnt(8)\ns_add_u32 s61,s61,12288\nMM v[224:227],a[32:35],v[28:31],v[224:227]\n"
"TR a[32:33],v5 offset:0\nTR a[34:35],v5 offset:4096\nMM v[228:231],a[36:39],v[28:31],v[228:231]\nTR a[36:37],v6 offset:0\nTR a[38:39],v6 offset:4096\n"
"MM v[232:235],a[40:43],v[28:31],v[232:235]\nTR a[40:41],v5 offset:512\nTR a[42:43],v5 offset:4608\nMM v[236:239],a[44:47],v[28:31],v[236:239]\n"
"TR a[44:45],v6 offset:512\nTR a[46:47],v6 offset:4608\nMM v[240:243],a[48:51],v[28:31],v[240:243]\nTR a[48:49],v5 offset:1024\n"
"TR a[50:51],v5 offset:5120\nMM v[244:247],a[52:55],v[28:31],v[244:247]\nTR a[52:53],v6 offset:1024\nTR a[54:55],v6 offset:5120\n"
"MM v[248:251],a[56:59],v[28:31],v[248:251]\nTR a[56:57],v5 offset:1536\nTR a[58:59],v5 offset:5632\nMM v[252:255],a[60:63],v[28:31],v[252:255]\n"
"TR a[60:61],v6 offset:1536\nTR a[62:63],v6 offset:5632\nMM a[116:119],a[124:127],v[28:31],a[116:119]\ns_and_b32 s61,s61,0x3ffff\n"
"MM v[20:23],a[0:3],a[160:163],v[40:43]\nv_exp_f32 v12,v12\nMM v[20:23],a[4:7],a[164:167],v[20:23]\ns_cmp_gt_u32 s72,5\ns_cbranch_scc1 L_sl0_5_%=\n"
"L_sl0_5b_%=:\nMM v[20:23],a[8:11],a[168:171],v[20:23]\nv_exp_f32 v13,v13\nMM v[20:23],a[12:15],a[172:175],v[20:23]\nCV v28,v12,v13\n"
"MM v[24:27],a[28:31],a[172:175],v[40:43]\nv_exp_f32 v14,v14\nMM v[24:27],a[24:27],a[168:171],v[24:27]\nMM v[24:27],a[20:23],a[164:167],v[24:27]\n"
"v_exp_f32 v15,v15\nMM v[24:27],a[16:19],a[160:163],v[24:27]\nCV v29,v14,v15\ns_waitcnt lgkmcnt(0)\nMM a[64:67],a[32:35],v[32:35],a[64:67]\n"
"v_exp_f32 v16,v16\nMM a[68:71],a[36:39],v[32:35],a[68:71]\nMM a[72:75],a[40:43],v[32:35],a[72:75]\nv_exp_f32 v17,v17\n"
"MM a[76:79],a[44:47],v[32:35],a[76:79]\nCV v30,v16,v17\nMM a[80:83],a[48:51],v[32:35],a[80:83]\nv_exp_f32 v18,v18\n"
"MM a[84:87],a[52:55],v[32:35],a[84:87]\nMM a[88:91],a[56:59],v[32:35],a[88:91]\nv_exp_f32 v19,v19\nMM a[92:95],a[60:63],v[32:35],a[92:95]\n"
"CV v31,v18,v19\nMM a[120:123],a[124:127],v[32:35],a[120:123]\ns_barrier\nMM v[12:15],a[0:3],a[176:179],v[44:47]\nv_exp_f32 v20,v20\n"
"MM v[12:15],a[4:7],a[180:183],v[12:15]\ns_memrealtime s[68:69]\ns_add_u32 m0,s63,32768\nMM v[12:15],a[8:11],a[184:187],v[12:15]\nv_exp_f32 v21,v21\n"
"MM v[12:15],a[12:15],a[188:191],v[12:15]\nCV v32,v20,v21\nMM v[16:19],a[28:31],a[188:191],v[44:47]\nv_exp_f32 v22,v22\n"
"MM v[16:19],a[24:27],a[184:187],v[16:19]\ns_cmp_gt_u32 s72,1\ns_cbranch_scc1 L_sl1_1_%=\nL_sl1_1b_%=:\nMM v[16:19],a[20:23],a[180:183],v[16:19]\n"
"v_exp_f32 v23,v23\nMM v[16:19],a[16:19],a[176:179],v[16:19]\nCV v33,v22,v23\nMM v[64:67],a[32:35],v[28:31],v[64:67]\nv_exp_f32 v24,v24\n"
"MM v[68:71],a[36:39],v[28:31],v[68:71]\nbuffer_load_dwordx4 v11,s[52:55],s62 offen offset:0 nt lds\nMM v[72:75],a[40:43],v[28:31],v[72:75]\n"
"v_exp_f32 v25,v25\nMM v[76:79],a[44:47],v[28:31],v[76:79]\nCV v34,v24,v25\nMM v[80:83],a[48:51],v[28:31],v[80:83]\nv_exp_f32 v26,v26\n"
"MM v[84:87],a[52:55],v[28:31],v[84:87]\nbuffer_load_dwordx4 v0,s[52:55],s62 offen offset:1024 nt lds\nMM v[88:91],a[56:59],v[28:31],v[88:91]\n"
"v_exp_f32 v27,v27\nMM v[92:95],a[60:63],v[28:31],v[92:95]\nCV v35,v26,v27\nMM a[96:99],a[124:127],v[28:31],a[96:99]\n"
"MM v[20:23],a[0:3],a[192:195],v[48:51]\nv_exp_f32 v12,v12\nMM v[20:23],a[4:7],a[196:199],v[20:23]\n"
"buffer_load_dwordx4 v11,s[52:55],s62 offen offset:2048 nt lds\nMM v[20:23],a[8:11],a[200:203],v[20:23]\nv_exp_f32 v13,v13\n"
"MM v[20:23],a[12:15],a[204:207],v[20:23]\nCV v28,v12,v13\nMM v[24:27],a[28:31],a[204:207],v[48:51]\nv_exp_f32 v14,v14\n"
"MM v[24:27],a[24:27],a[200:203],v[24:27]\nv_xor_b32 v1,8192,v1\nv_xor_b32 v2,8192,v2\nMM v[24:27],a[20:23],a[196:199],v[24:27]\nv_exp_f32 v15,v15\n"
"MM v[24:27],a[16:19],a[192:195],v[24:27]\nCV v29,v14,v15\nv_xor_b32 v3,8192,v3\nMM v[96:99],a[32:35],v[32:35],v[96:99]\nv_exp_f32 v16,v16\n"
"MM v[100:103],a[36:39],v[32:35],v[100:103]\nv_xor_b32 v4,8192,v4\ns_waitcnt lgkmcnt(0)\ns_sub_i32 s81,s70,s68\n"
"MM v[104:107],a[40:43],v[32:35],v[104:107]\nv_exp_f32 v17,v17\nMM v[108:111],a[44:47],v[32:35],v[108:111]\nCV v30,v16,v17\n"
"buffer_load_dwordx4 v0,s[52:55],s62 offen offset:3072 nt lds\nMM v[112:115],a[48:51],v[32:35],v[112:115]\nv_exp_f32 v18,v18\n"
"MM v[116:119],a[52:55],v[32:35],v[116:119]\ns_add_i32 s80,s81,60\ns_cmp_lt_i32 s80,s89\ns_cselect_b32 s81,s85,s81\n"
"MM v[120:123],a[56:59],v[32:35],v[120:123]\nv_exp_f32 v19,v19\nMM v[124:127],a[60:63],v[32:35],v[124:127]\nCV v31,v18,v19\ns_add_u32 s62,s62,4096\n"
"MM a[100:103],a[124:127],v[32:35],a[100:103]\ns_sub_i32 s89,s89,30\ns_max_i32 s89,s89,s81\nMM v[12:15],a[0:3],a[208:211],v[52:55]\nv_exp_f32 v20,v20\n"
"MM v[12:15],a[4:7],a[212:215],v[12:15]\ns_max_i32 s89,s89,s93\ns_add_u32 s70,s68,s89\nMM v[12:15],a[8:11],a[216:219],v[12:15]\nv_exp_f32 v21,v21\n"
"MM v[12:15],a[12:15],a[220:223],v[12:15]\nCV v32,v20,v21\ns_and_b32 s62,s62,0x3ffff\nMM v[16:19],a[28:31],a[220:223],v[52:55]\nv_exp_f32 v22,v22\n"
"MM v[16:19],a[24:27],a[216:219],v[16:19]\ns_add_u32 s70,s70,s71\ns_lshr_b32 s72,s89,3\nMM v[16:19],a[20:23],a[212:215],v[16:19]\nv_exp_f32 v23,v23\n"
"MM v[16:19],a[16:19],a[208:211],v[16:19]\nCV v33,v22,v23\nMM v[128:131],a[32:35],v[28:31],v[128:131]\nv_exp_f32 v24,v24\n"
"MM v[132:135],a[36:39],v[28:31],v[132:135]\ns_cmp_gt_u32 s72,4\ns_cbranch_scc1 L_sl1_4_%=\nL_sl1_4b_%=:\nMM v[136:139],a[40:43],v[28:31],v[136:139]\n"
"v_exp_f32 v25,v25\nMM v[140:143],a[44:47],v[28:31],v[140:143]\nCV v34,v24,v25\ns_add_u32 m0,s63,36864\nMM v[144:147],a[48:51],v[28:31],v[144:147]\n"
"v_exp_f32 v26,v26\nMM v[148:151],a[52:55],v[28:31],v[148:151]\nMM v[152:155],a[56:59],v[28:31],v[152:155]\nv_exp_f32 v27,v27\n"
"MM v[156:159],a[60:63],v[28:31],v[156:159]\nCV v35,v26,v27\nMM a[104:107],a[124:127],v[28:31],a[104:107]\n"
"buffer_load_dwordx4 v11,s[52:55],s62 offen offset:0 nt lds\nMM v[20:23],a[0:3],a[224:227],v[56:59]\nv_exp_f32 v12,v12\n"
"MM v[20:23],a[4:7],a[228:231],v[20:23]\nds_read_b128 a[0:3],v1 offset:0\nds_read_b128 a[4:7],v2 offset:0\nMM v[20:23],a[8:11],a[232:235],v[20:23]\n"
"v_exp_f32 v13,v13\nMM v[20:23],a[12:15],a[236:239],v[20:23]\nCV v28,v12,v13\nds_read_b128 a[8:11],v3 offset:0\n"
"MM v[24:27],a[28:31],a[236:239],v[56:59]\nv_exp_f32 v14,v14\nMM v[24:27],a[24:27],a[232:235],v[24:27]\nds_read_b128 a[12:15],v4 offset:0\n"
"ds_read_b128 a[28:31],v4 offset:4096\nMM v[24:27],a[20:23],a[228:231],v[24:27]\nv_exp_f32 v15,v15\nMM v[24:27],a[16:19],a[224:227],v[24:27]\n"
"CV v29,v14,v15\nds_read_b128 a[24:27],v3 offset:4096\nMM v[160:163],a[32:35],v[32:35],v[160:163]\nv_exp_f32 v16,v16\n"
"MM v[164:167],a[36:39],v[32:35],v[164:167]\nds_read_b128 a[20:23],v2 offset:4096\nds_read_b128 a[16:19],v1 offset:4096\n"
"MM v[168:171],a[40:43],v[32:35],v[168:171]\nv_exp_f32 v17,v17\nMM v[172:175],a[44:47],v[32:35],v[172:175]\nCV v30,v16,v17\n"
"MM v[176:179],a[48:51],v[32:35],v[176:179]\nv_exp_f32 v18,v18\nMM v[180:183],a[52:55],v[32:35],v[180:183]\n"
"buffer_load_dwordx4 v0,s[52:55],s62 offen offset:1024 nt lds\nMM v[184:187],a[56:59],v[32:35],v[184:187]\nv_exp_f32 v19,v19\n"
"MM v[188:191],a[60:63],v[32:35],v[188:191]\nCV v31,v18,v19\nMM a[108:111],a[124:127],v[32:35],a[108:111]\n"
"buffer_load_dwordx4 v11,s[52:55],s62 offen offset:2048 nt lds\ns_waitcnt lgkmcnt(0)\nMM v[12:15],a[0:3],a[144:147],v[36:39]\nv_exp_f32 v20,v20\n"
"MM v[12:15],a[4:7],a[148:151],v[12:15]\ns_cmp_gt_u32 s72,3\ns_cbranch_scc1 L_sl1_3_%=\nL_sl1_3b_%=:\nMM v[12:15],a[8:11],a[152:155],v[12:15]\n"
"v_exp_f32 v21,v21\nMM v[12:15],a[12:15],a[156:159],v[12:15]\nCV v32,v20,v21\nMM v[16:19],a[28:31],a[156:159],v[36:39]\nv_exp_f32 v22,v22\n"
"MM v[16:19],a[24:27],a[152:155],v[16:19]\nbuffer_load_dwordx4 v0,s[52:55],s62 offen offset:3072 nt lds\nMM v[16:19],a[20:23],a[148:151],v[16:19]\n"
"v_exp_f32 v23,v23\nMM v[16:19],a[16:19],a[144:147],v[16:19]\nCV v33,v22,v23\ns_add_u32 s62,s62,12288\nMM v[192:195],a[32:35],v[28:31],v[192:195]\n"
"v_exp_f32 v24,v24\nMM v[196:199],a[36:39],v[28:31],v[196:199]\nv_exp_f32 v12,v12\nMM v[200:203],a[40:43],v[28:31],v[200:203]\nv_exp_f32 v25,v25\n"
"MM v[204:207],a[44:47],v[28:31],v[204:207]\nCV v34,v24,v25\nv_xor_b32 v5,8192,v5\nMM v[208:211],a[48:51],v[28:31],v[208:211]\nv_exp_f32 v26,v26\n"
"MM v[212:215],a[52:55],v[28:31],v[212:215]\nv_exp_f32 v13,v13\nMM v[216:219],a[56:59],v[28:31],v[216:219]\nv_exp_f32 v27,v27\n"
"MM v[220:223],a[60:63],v[28:31],v[220:223]\nCV v35,v26,v27\nv_xor_b32 v6,8192,v6\nMM a[112:115],a[124:127],v[28:31],a[112:115]\nv_exp_f32 v14,v14\n"
"MM v[20:23],a[0:3],a[160:163],v[40:43]\nv_exp_f32 v15,v15\nMM v[20:23],a[4:7],a[164:167],v[20:23]\nCV v28,v12,v13\nCV v29,v14,v15\n"
"MM v[20:23],a[8:11],a[168:171],v[20:23]\nv_exp_f32 v16,v16\nMM v[20:23],a[12:15],a[172:175],v[20:23]\nv_exp_f32 v17,v17\n"
"MM v[24:27],a[28:31],a[172:175],v[40:43]\nv_exp_f32 v18,v18\nMM v[24:27],a[24:27],a[168:171],v[24:27]\nv_exp_f32 v19,v19\n"
"MM v[24:27],a[20:23],a[164:167],v[24:27]\nCV v30,v16,v17\nCV v31,v18,v19\nMM v[24:27],a[16:19],a[160:163],v[24:27]\ns_and_b32 s62,s62,0x3ffff\n"
"MM v[224:227],a[32:35],v[32:35],v[224:227]\nTR a[32:33],v5 offset:0\nTR a[34:35],v5 offset:4096\nMM v[228:231],a[36:39],v[32:35],v[228:231]\n"
"TR a[36:37],v6 offset:0\nTR a[38:39],v6 offset:4096\nMM v[232:235],a[40:43],v[32:35],v[232:235]\nTR a[40:41],v5 offset:512\nTR a[42:43],v5 offset:4608\n"
"MM v[236:239],a[44:47],v[32:35],v[236:239]\nTR a[44:45],v6 offset:512\nTR a[46:47],v6 offset:4608\nMM v[240:243],a[48:51],v[32:35],v[240:243]\n"
"TR a[48:49],v5 offset:1024\nTR a[50:51],v5 offset:5120\nMM v[244:247],a[52:55],v[32:35],v[244:247]\nTR a[52:53],v6 offset:1024\n"
"TR a[54:55],v6 offset:5120\nMM v[248:251],a[56:59],v[32:35],v[248:251]\nTR a[56:57],v5 offset:1536\nTR a[58:59],v5 offset:5632\n"
"MM v[252:255],a[60:63],v[32:35],v[252:255]\nTR a[60:61],v6 offset:1536\nTR a[62:63],v6 offset:5632\nMM a[116:119],a[124:127],v[32:35],a[116:119]\n"
"MM v[12:15],a[0:3],a[176:179],v[44:47]\nv_exp_f32 v20,v20\nMM v[12:15],a[4:7],a[180:183],v[12:15]\ns_cmp_gt_u32 s72,6\ns_cbranch_scc1 L_sl1_6_%=\n"
"L_sl1_6b_%=:\nMM v[12:15],a[8:11],a[184:187],v[12:15]\nv_exp_f32 v21,v21\nMM v[12:15],a[12:15],a[188:191],v[12:15]\nCV v32,v20,v21\ns_add_u32 m0,s64,0\n"
"MM v[16:19],a[28:31],a[188:191],v[44:47]\nv_exp_f32 v22,v22\nMM v[16:19],a[24:27],a[184:187],v[16:19]\nMM v[16:19],a[20:23],a[180:183],v[16:19]\n"
"v_exp_f32 v23,v23\nMM v[16:19],a[16:19],a[176:179],v[16:19]\nCV v33,v22,v23\ns_waitcnt lgkmcnt(0)\nMM v[64:67],a[32:35],v[28:31],v[64:67]\n"
"v_exp_f32 v24,v24\nMM v[68:71],a[36:39],v[28:31],v[68:71]\nbuffer_load_dwordx4 v7,s[48:51],s61 offen offset:0 nt lds\n"
"MM v[72:75],a[40:43],v[28:31],v[72:75]\nv_exp_f32 v25,v25\nMM v[76:79],a[44:47],v[28:31],v[76:79]\nCV v34,v24,v25\n"
"MM v[80:83],a[48:51],v[28:31],v[80:83]\nv_exp_f32 v26,v26\nMM v[84:87],a[52:55],v[28:31],v[84:87]\ns_xor_b32 s91,s65,s66\ns_or_b32 s91,s91,8192\n"
"MM v[88:91],a[56:59],v[28:31],v[88:91]\nv_exp_f32 v27,v27\nMM v[92:95],a[60:63],v[28:31],v[92:95]\nCV v35,v26,v27\nv_xor_b32 v1,s91,v1\n"
"MM a[96:99],a[124:127],v[28:31],a[96:99]\nv_xor_b32 v2,s91,v2\nv_xor_b32 v3,s91,v3\nMM v[20:23],a[0:3],a[192:195],v[48:51]\nv_exp_f32 v12,v12\n"
"MM v[20:23],a[4:7],a[196:199],v[20:23]\nv_xor_b32 v4,s91,v4\nMM v[20:23],a[8:11],a[200:203],v[20:23]\nv_exp_f32 v13,v13\n"
"MM v[20:23],a[12:15],a[204:207],v[20:23]\nCV v28,v12,v13\nMM v[24:27],a[28:31],a[204:207],v[48:51]\nv_exp_f32 v14,v14\n"
"MM v[24:27],a[24:27],a[200:203],v[24:27]\ns_cmp_gt_u32 s72,0\ns_cbranch_scc1 L_sl1_0_%=\nL_sl1_0b_%=:\nMM v[24:27],a[20:23],a[196:199],v[24:27]\n"
"v_exp_f32 v15,v15\nMM v[24:27],a[16:19],a[192:195],v[24:27]\nCV v29,v14,v15\nMM v[96:99],a[32:35],v[32:35],v[96:99]\nv_exp_f32 v16,v16\n"
"MM v[100:103],a[36:39],v[32:35],v[100:103]\nbuffer_load_dwordx4 v8,s[48:51],s61 offen offset:1024 nt lds\nMM v[104:107],a[40:43],v[32:35],v[104:107]\n"
"v_exp_f32 v17,v17\nMM v[108:111],a[44:47],v[32:35],v[108:111]\nCV v30,v16,v17\nMM v[112:115],a[48:51],v[32:35],v[112:115]\nv_exp_f32 v18,v18\n"
"MM v[116:119],a[52:55],v[32:35],v[116:119]\nbuffer_load_dwordx4 v9,s[48:51],s61 offen offset:2048 nt lds\nMM v[120:123],a[56:59],v[32:35],v[120:123]\n"
"v_exp_f32 v19,v19\nMM v[124:127],a[60:63],v[32:35],v[124:127]\nCV v31,v18,v19\nMM a[100:103],a[124:127],v[32:35],a[100:103]\n"
"buffer_load_dwordx4 v10,s[48:51],s61 offen offset:3072 nt lds\nMM v[12:15],a[0:3],a[208:211],v[52:55]\nv_exp_f32 v20,v20\n"
"MM v[12:15],a[4:7],a[212:215],v[12:15]\ns_cmp_gt_u32 s72,7\ns_cbranch_scc1 L_sl1_7_%=\nL_sl1_7b_%=:\nMM v[12:15],a[8:11],a[216:219],v[12:15]\n"
"v_exp_f32 v21,v21\nMM v[12:15],a[12:15],a[220:223],v[12:15]\nCV v32,v20,v21\ns_add_u32 s61,s61,4096\nMM v[16:19],a[28:31],a[220:223],v[52:55]\n"
"v_exp_f32 v22,v22\nMM v[16:19],a[24:27],a[216:219],v[16:19]\ns_and_b32 s61,s61,0x3ffff\nMM v[16:19],a[20:23],a[212:215],v[16:19]\nv_exp_f32 v23,v23\n"
"MM v[16:19],a[16:19],a[208:211],v[16:19]\nCV v33,v22,v23\nMM v[128:131],a[32:35],v[28:31],v[128:131]\nv_exp_f32 v24,v24\n"
"MM v[132:135],a[36:39],v[28:31],v[132:135]\nMM v[136:139],a[40:43],v[28:31],v[136:139]\nv_exp_f32 v25,v25\nMM v[140:143],a[44:47],v[28:31],v[140:143]\n"
"CV v34,v24,v25\nMM v[144:147],a[48:51],v[28:31],v[144:147]\nv_exp_f32 v26,v26\nMM v[148:151],a[52:55],v[28:31],v[148:151]\ns_add_u32 m0,s64,4096\n"
"MM v[152:155],a[56:59],v[28:31],v[152:155]\nv_exp_f32 v27,v27\nMM v[156:159],a[60:63],v[28:31],v[156:159]\nCV v35,v26,v27\n"
"MM a[104:107],a[124:127],v[28:31],a[104:107]\ns_waitcnt vmcnt(12)\nMM v[20:23],a[0:3],a[224:227],v[56:59]\nv_exp_f32 v12,v12\n"
"MM v[20:23],a[4:7],a[228:231],v[20:23]\nds_read_b128 a[0:3],v1 offset:0\nds_read_b128 a[4:7],v2 offset:0\nMM v[20:23],a[8:11],a[232:235],v[20:23]\n"
"v_exp_f32 v13,v13\nMM v[20:23],a[12:15],a[236:239],v[20:23]\nCV v28,v12,v13\nds_read_b128 a[8:11],v3 offset:0\n"
"MM v[24:27],a[28:31],a[236:239],v[56:59]\nv_exp_f32 v14,v14\nMM v[24:27],a[24:27],a[232:235],v[24:27]\nds_read_b128 a[12:15],v4 offset:0\n"
"ds_read_b128 a[28:31],v4 offset:4096\nMM v[24:27],a[20:23],a[228:231],v[24:27]\nv_exp_f32 v15,v15\nMM v[24:27],a[16:19],a[224:227],v[24:27]\n"
"CV v29,v14,v15\nds_read_b128 a[24:27],v3 offset:4096\nMM v[160:163],a[32:35],v[32:35],v[160:163]\nv_exp_f32 v16,v16\n"
"MM v[164:167],a[36:39],v[32:35],v[164:167]\nds_read_b128 a[20:23],v2 offset:4096\nds_read_b128 a[16:19],v1 offset:4096\n"
"MM v[168:171],a[40:43],v[32:35],v[168:171]\nv_exp_f32 v17,v17\nMM v[172:175],a[44:47],v[32:35],v[172:175]\nCV v30,v16,v17\n"
"MM v[176:179],a[48:51],v[32:35],v[176:179]\nv_exp_f32 v18,v18\nMM v[180:183],a[52:55],v[32:35],v[180:183]\ns_cmp_gt_u32 s72,2\n"
"s_cbranch_scc1 L_sl1_2_%=\nL_sl1_2b_%=:\nMM v[184:187],a[56:59],v[32:35],v[184:187]\nv_exp_f32 v19,v19\nMM v[188:191],a[60:63],v[32:35],v[188:191]\n"
"CV v31,v18,v19\nMM a[108:111],a[124:127],v[32:35],a[108:111]\nbuffer_load_dwordx4 v7,s[48:51],s61 offen offset:0 nt lds\ns_waitcnt lgkmcnt(0)\n"
"MM v[12:15],a[0:3],a[240:243],v[60:63]\nv_exp_f32 v20,v20\nMM v[12:15],a[4:7],a[244:247],v[12:15]\n"
"buffer_load_dwordx4 v8,s[48:51],s61 offen offset:1024 nt lds\nMM v[12:15],a[8:11],a[248:251],v[12:15]\nv_exp_f32 v21,v21\n"
"MM v[12:15],a[12:15],a[252:255],v[12:15]\nCV v32,v20,v21\nMM v[16:19],a[28:31],a[252:255],v[60:63]\nv_exp_f32 v22,v22\n"
"MM v[16:19],a[24:27],a[248:251],v[16:19]\nbuffer_load_dwordx4 v9,s[48:51],s61 offen offset:2048 nt lds\nMM v[16:19],a[20:23],a[244:247],v[16:19]\n"
"v_exp_f32 v23,v23\nMM v[16:19],a[16:19],a[240:243],v[16:19]\nCV v33,v22,v23\nbuffer_load_dwordx4 v10,s[48:51],s61 offen offset:3072 nt lds\n"
"MM v[192:195],a[32:35],v[28:31],v[192:195]\nv_exp_f32 v24,v24\nMM v[196:199],a[36:39],v[28:31],v[196:199]\nv_exp_f32 v12,v12\n"
"MM v[200:203],a[40:43],v[28:31],v[200:203]\nv_exp_f32 v25,v25\nMM v[204:207],a[44:47],v[28:31],v[204:207]\nCV v34,v24,v25\nv_xor_b32 v5,172032,v5\n"
"MM v[208:211],a[48:51],v[28:31],v[208:211]\nv_exp_f32 v26,v26\nMM v[212:215],a[52:55],v[28:31],v[212:215]\nv_exp_f32 v13,v13\n"
"MM v[216:219],a[56:59],v[28:31],v[216:219]\nv_exp_f32 v27,v27\nMM v[220:223],a[60:63],v[28:31],v[220:223]\nCV v35,v26,v27\nv_xor_b32 v6,172032,v6\n"
"MM a[112:115],a[124:127],v[28:31],a[112:115]\nv_exp_f32 v14,v14\nMM v[20:23],a[0:3],a[144:147],v[36:39]\nv_exp_f32 v15,v15\n"
"MM v[20:23],a[4:7],a[148:151],v[20:23]\nCV v28,v12,v13\nCV v29,v14,v15\nMM v[20:23],a[8:11],a[152:155],v[20:23]\nv_exp_f32 v16,v16\n"
"MM v[20:23],a[12:15],a[156:159],v[20:23]\nv_exp_f32 v17,v17\nMM v[24:27],a[28:31],a[156:159],v[36:39]\nv_exp_f32 v18,v18\n"
"MM v[24:27],a[24:27],a[152:155],v[24:27]\nv_exp_f32 v19,v19\nMM v[24:27],a[20:23],a[148:151],v[24:27]\nCV v30,v16,v17\nCV v31,v18,v19\n"
"MM v[24:27],a[16:19],a[144:147],v[24:27]\ns_waitcnt vmcnt(8)\ns_add_u32 s61,s61,12288\nMM v[224:227],a[32:35],v[32:35],v[224:227]\n"
"TR a[32:33],v5 offset:0\nTR a[34:35],v5 offset:4096\nMM v[228:231],a[36:39],v[32:35],v[228:231]\nTR a[36:37],v6 offset:0\nTR a[38:39],v6 offset:4096\n"
"MM v[232:235],a[40:43],v[32:35],v[232:235]\nTR a[40:41],v5 offset:512\nTR a[42:43],v5 offset:4608\nMM v[236:239],a[44:47],v[32:35],v[236:239]\n"
"TR a[44:45],v6 offset:512\nTR a[46:47],v6 offset:4608\nMM v[240:243],a[48:51],v[32:35],v[240:243]\nTR a[48:49],v5 offset:1024\n"
"TR a[50:51],v5 offset:5120\nMM v[244:247],a[52:55],v[32:35],v[244:247]\nTR a[52:53],v6 offset:1024\nTR a[54:55],v6 offset:5120\n"
"MM v[248:251],a[56:59],v[32:35],v[248:251]\nTR a[56:57],v5 offset:1536\nTR a[58:59],v5 offset:5632\nMM v[252:255],a[60:63],v[32:35],v[252:255]\n"
"TR a[60:61],v6 offset:1536\nTR a[62:63],v6 offset:5632\nMM a[116:119],a[124:127],v[32:35],a[116:119]\ns_and_b32 s61,s61,0x3ffff\n"
"MM v[12:15],a[0:3],a[160:163],v[40:43]\nv_exp_f32 v20,v20\nMM v[12:15],a[4:7],a[164:167],v[12:15]\ns_cmp_gt_u32 s72,5\ns_cbranch_scc1 L_sl1_5_%=\n"
"L_sl1_5b_%=:\nMM v[12:15],a[8:11],a[168:171],v[12:15]\nv_exp_f32 v21,v21\nMM v[12:15],a[12:15],a[172:175],v[12:15]\nCV v32,v20,v21\n"
"MM v[16:19],a[28:31],a[172:175],v[40:43]\nv_exp_f32 v22,v22\nMM v[16:19],a[24:27],a[168:171],v[16:19]\nMM v[16:19],a[20:23],a[164:167],v[16:19]\n"
"v_exp_f32 v23,v23\nMM v[16:19],a[16:19],a[160:163],v[16:19]\nCV v33,v22,v23\ns_waitcnt lgkmcnt(0)\nMM a[64:67],a[32:35],v[28:31],a[64:67]\n"
"v_exp_f32 v24,v24\nMM a[68:71],a[36:39],v[28:31],a[68:71]\ns_mov_b32 s86,s65\ns_mov_b32 s65,s64\nMM a[72:75],a[40:43],v[28:31],a[72:75]\n"
"v_exp_f32 v25,v25\nMM a[76:79],a[44:47],v[28:31],a[76:79]\nCV v34,v24,v25\nMM a[80:83],a[48:51],v[28:31],a[80:83]\nv_exp_f32 v26,v26\n"
"MM a[84:87],a[52:55],v[28:31],a[84:87]\ns_mov_b32 s64,s66\ns_mov_b32 s66,s86\nMM a[88:91],a[56:59],v[28:31],a[88:91]\nv_exp_f32 v27,v27\n"
"MM a[92:95],a[60:63],v[28:31],a[92:95]\nCV v35,v26,v27\nMM a[120:123],a[124:127],v[28:31],a[120:123]\nL_mid_%=:\ns_barrier\n"
"MM v[20:23],a[0:3],a[176:179],v[44:47]\nv_exp_f32 v12,v12\nMM v[20:23],a[4:7],a[180:183],v[20:23]\ns_memrealtime s[68:69]\ns_add_u32 m0,s63,131072\n"
"MM v[20:23],a[8:11],a[184:187],v[20:23]\nv_exp_f32 v13,v13\nMM v[20:23],a[12:15],a[188:191],v[20:23]\nCV v28,v12,v13\n"
"MM v[24:27],a[28:31],a[188:191],v[44:47]\nv_exp_f32 v14,v14\nMM v[24:27],a[24:27],a[184:187],v[24:27]\ns_cmp_gt_u32 s72,1\ns_cbranch_scc1 L_sl0_1_%=\n"
"L_sl0_1b_%=:\nMM v[24:27],a[20:23],a[180:183],v[24:27]\nv_exp_f32 v15,v15\nMM v[24:27],a[16:19],a[176:179],v[24:27]\nCV v29,v14,v15\n"
"MM v[64:67],a[32:35],v[32:35],v[64:67]\nv_exp_f32 v16,v16\nMM v[68:71],a[36:39],v[32:35],v[68:71]\n"
"buffer_load_dwordx4 v11,s[52:55],s62 offen offset:0 nt lds\nMM v[72:75],a[40:43],v[32:35],v[72:75]\nv_exp_f32 v17,v17\n"
"MM v[76:79],a[44:47],v[32:35],v[76:79]\nCV v30,v16,v17\nMM v[80:83],a[48:51],v[32:35],v[80:83]\nv_exp_f32 v18,v18\n"
"MM v[84:87],a[52:55],v[32:35],v[84:87]\nbuffer_load_dwordx4 v0,s[52:55],s62 offen offset:1024 nt lds\nMM v[88:91],a[56:59],v[32:35],v[88:91]\n"
"v_exp_f32 v19,v19\nMM v[92:95],a[60:63],v[32:35],v[92:95]\nCV v31,v18,v19\nMM a[96:99],a[124:127],v[32:35],a[96:99]\n"
"MM v[12:15],a[0:3],a[192:195],v[48:51]\nv_exp_f32 v20,v20\nMM v[12:15],a[4:7],a[196:199],v[12:15]\n"
"buffer_load_dwordx4 v11,s[52:55],s62 offen offset:2048 nt lds\nMM v[12:15],a[8:11],a[200:203],v[12:15]\nv_exp_f32 v21,v21\n"
"MM v[12:15],a[12:15],a[204:207],v[12:15]\nCV v32,v20,v21\nMM v[16:19],a[28:31],a[204:207],v[48:51]\nv_exp_f32 v22,v22\n"
"MM v[16:19],a[24:27],a[200:203],v[16:19]\nv_xor_b32 v1,8192,v1\nv_xor_b32 v2,8192,v2\nMM v[16:19],a[20:23],a[196:199],v[16:19]\nv_exp_f32 v23,v23\n"
"MM v[16:19],a[16:19],a[192:195],v[16:19]\nCV v33,v22,v23\nv_xor_b32 v3,8192,v3\nMM v[96:99],a[32:35],v[28:31],v[96:99]\nv_exp_f32 v24,v24\n"
"MM v[100:103],a[36:39],v[28:31],v[100:103]\nv_xor_b32 v4,8192,v4\ns_waitcnt lgkmcnt(0)\ns_sub_i32 s81,s70,s68\n"
"MM v[104:107],a[40:43],v[28:31],v[104:107]\nv_exp_f32 v25,v25\nMM v[108:111],a[44:47],v[28:31],v[108:111]\nCV v34,v24,v25\n"
"buffer_load_dwordx4 v0,s[52:55],s62 offen offset:3072 nt lds\nMM v[112:115],a[48:51],v[28:31],v[112:115]\nv_exp_f32 v26,v26\n"
"MM v[116:119],a[52:55],v[28:31],v[116:119]\ns_add_i32 s80,s81,60\ns_cmp_lt_i32 s80,s89\ns_cselect_b32 s81,s85,s81\n"
"MM v[120:123],a[56:59],v[28:31],v[120:123]\nv_exp_f32 v27,v27\nMM v[124:127],a[60:63],v[28:31],v[124:127]\nCV v35,v26,v27\ns_add_u32 s62,s62,4096\n"
"MM a[100:103],a[124:127],v[28:31],a[100:103]\ns_sub_i32 s89,s89,30\ns_max_i32 s89,s89,s81\nMM v[20:23],a[0:3],a[208:211],v[52:55]\nv_exp_f32 v12,v12\n"
"MM v[20:23],a[4:7],a[212:215],v[20:23]\ns_max_i32 s89,s89,s93\ns_add_u32 s70,s68,s89\nMM v[20:23],a[8:11],a[216:219],v[20:23]\nv_exp_f32 v13,v13\n"
"MM v[20:23],a[12:15],a[220:223],v[20:23]\nCV v28,v12,v13\ns_and_b32 s62,s62,0x3ffff\nMM v[24:27],a[28:31],a[220:223],v[52:55]\nv_exp_f32 v14,v14\n"
"MM v[24:27],a[24:27],a[216:219],v[24:27]\ns_add_u32 s70,s70,s71\ns_lshr_b32 s72,s89,3\nMM v[24:27],a[20:23],a[212:215],v[24:27]\nv_exp_f32 v15,v15\n"
"MM v[24:27],a[16:19],a[208:211],v[24:27]\nCV v29,v14,v15\nMM v[128:131],a[32:35],v[32:35],v[128:131]\nv_exp_f32 v16,v16\n"
"MM v[132:135],a[36:39],v[32:35],v[132:135]\ns_cmp_gt_u32 s72,4\ns_cbranch_scc1 L_sl0_4_%=\nL_sl0_4b_%=:\nMM v[136:139],a[40:43],v[32:35],v[136:139]\n"
"v_exp_f32 v17,v17\nMM v[140:143],a[44:47],v[32:35],v[140:143]\nCV v30,v16,v17\ns_add_u32 m0,s63,135168\nMM v[144:147],a[48:51],v[32:35],v[144:147]\n"
"v_exp_f32 v18,v18\nMM v[148:151],a[52:55],v[32:35],v[148:151]\nMM v[152:155],a[56:59],v[32:35],v[152:155]\nv_exp_f32 v19,v19\n"
"MM v[156:159],a[60:63],v[32:35],v[156:159]\nCV v31,v18,v19\nMM a[104:107],a[124:127],v[32:35],a[104:107]\n"
"buffer_load_dwordx4 v11,s[52:55],s62 offen offset:0 nt lds\nMM v[12:15],a[0:3],a[224:227],v[56:59]\nv_exp_f32 v20,v20\n"
"MM v[12:15],a[4:7],a[228:231],v[12:15]\nds_read_b128 a[0:3],v1 offset:0\nds_read_b128 a[4:7],v2 offset:0\nMM v[12:15],a[8:11],a[232:235],v[12:15]\n"
"v_exp_f32 v21,v21\nMM v[12:15],a[12:15],a[236:239],v[12:15]\nCV v32,v20,v21\nds_read_b128 a[8:11],v3 offset:0\n"
"MM v[16:19],a[28:31],a[236:239],v[56:59]\nv_exp_f32 v22,v22\nMM v[16:19],a[24:27],a[232:235],v[16:19]\nds_read_b128 a[12:15],v4 offset:0\n"
"ds_read_b128 a[28:31],v4 offset:4096\nMM v[16:19],a[20:23],a[228:231],v[16:19]\nv_exp_f32 v23,v23\nMM v[16:19],a[16:19],a[224:227],v[16:19]\n"
"CV v33,v22,v23\nds_read_b128 a[24:27],v3 offset:4096\nMM v[160:163],a[32:35],v[28:31],v[160:163]\nv_exp_f32 v24,v24\n"
"MM v[164:167],a[36:39],v[28:31],v[164:167]\nds_read_b128 a[20:23],v2 offset:4096\nds_read_b128 a[16:19],v1 offset:4096\n"
"MM v[168:171],a[40:43],v[28:31],v[168:171]\nv_exp_f32 v25,v25\nMM v[172:175],a[44:47],v[28:31],v[172:175]\nCV v34,v24,v25\n"
"MM v[176:179],a[48:51],v[28:31],v[176:179]\nv_exp_f32 v26,v26\nMM v[180:183],a[52:55],v[28:31],v[180:183]\n"
"buffer_load_dwordx4 v0,s[52:55],s62 offen offset:1024 nt lds\nMM v[184:187],a[56:59],v[28:31],v[184:187]\nv_exp_f32 v27,v27\n"
"MM v[188:191],a[60:63],v[28:31],v[188:191]\nCV v35,v26,v27\nMM a[108:111],a[124:127],v[28:31],a[108:111]\n"
"buffer_load_dwordx4 v11,s[52:55],s62 offen offset:2048 nt lds\ns_waitcnt lgkmcnt(0)\nMM v[20:23],a[0:3],a[144:147],v[36:39]\nv_exp_f32 v12,v12\n"
"MM v[20:23],a[4:7],a[148:151],v[20:23]\ns_cmp_gt_u32 s72,3\ns_cbranch_scc1 L_sl0_3_%=\nL_sl0_3b_%=:\nMM v[20:23],a[8:11],a[152:155],v[20:23]\n"
"v_exp_f32 v13,v13\nMM v[20:23],a[12:15],a[156:159],v[20:23]\nCV v28,v12,v13\nMM v[24:27],a[28:31],a[156:159],v[36:39]\nv_exp_f32 v14,v14\n"
"MM v[24:27],a[24:27],a[152:155],v[24:27]\nbuffer_load_dwordx4 v0,s[52:55],s62 offen offset:3072 nt lds\nMM v[24:27],a[20:23],a[148:151],v[24:27]\n"
"v_exp_f32 v15,v15\nMM v[24:27],a[16:19],a[144:147],v[24:27]\nCV v29,v14,v15\ns_add_u32 s62,s62,12288\nMM v[192:195],a[32:35],v[32:35],v[192:195]\n"
"v_exp_f32 v16,v16\nMM v[196:199],a[36:39],v[32:35],v[196:199]\nv_exp_f32 v20,v20\nMM v[200:203],a[40:43],v[32:35],v[200:203]\nv_exp_f32 v17,v17\n"
"MM v[204:207],a[44:47],v[32:35],v[204:207]\nCV v30,v16,v17\nv_xor_b32 v5,8192,v5\nMM v[208:211],a[48:51],v[32:35],v[208:211]\nv_exp_f32 v18,v18\n"
"MM v[212:215],a[52:55],v[32:35],v[212:215]\nv_exp_f32 v21,v21\nMM v[216:219],a[56:59],v[32:35],v[216:219]\nv_exp_f32 v19,v19\n"
"MM v[220:223],a[60:63],v[32:35],v[220:223]\nCV v31,v18,v19\nv_xor_b32 v6,8192,v6\nMM a[112:115],a[124:127],v[32:35],a[112:115]\nv_exp_f32 v22,v22\n"
"MM v[12:15],a[0:3],a[160:163],v[40:43]\nv_exp_f32 v23,v23\nMM v[12:15],a[4:7],a[164:167],v[12:15]\nCV v32,v20,v21\nCV v33,v22,v23\n"
"MM v[12:15],a[8:11],a[168:171],v[12:15]\nv_exp_f32 v24,v24\nMM v[12:15],a[12:15],a[172:175],v[12:15]\nv_exp_f32 v25,v25\n"
"MM v[16:19],a[28:31],a[172:175],v[40:43]\nv_exp_f32 v26,v26\nMM v[16:19],a[24:27],a[168:171],v[16:19]\nv_exp_f32 v27,v27\n"
"MM v[16:19],a[20:23],a[164:167],v[16:19]\nCV v34,v24,v25\nCV v35,v26,v27\nMM v[16:19],a[16:19],a[160:163],v[16:19]\ns_and_b32 s62,s62,0x3ffff\n"
"MM v[224:227],a[32:35],v[28:31],v[224:227]\nTR a[32:33],v5 offset:0\nTR a[34:35],v5 offset:4096\nMM v[228:231],a[36:39],v[28:31],v[228:231]\n"
"TR a[36:37],v6 offset:0\nTR a[38:39],v6 offset:4096\nMM v[232:235],a[40:43],v[28:31],v[232:235]\nTR a[40:41],v5 offset:512\nTR a[42:43],v5 offset:4608\n"
"MM v[236:239],a[44:47],v[28:31],v[236:239]\nTR a[44:45],v6 offset:512\nTR a[46:47],v6 offset:4608\nMM v[240:243],a[48:51],v[28:31],v[240:243]\n"
"TR a[48:49],v5 offset:1024\nTR a[50:51],v5 offset:5120\nMM v[244:247],a[52:55],v[28:31],v[244:247]\nTR a[52:53],v6 offset:1024\n"
"TR a[54:55],v6 offset:5120\nMM v[248:251],a[56:59],v[28:31],v[248:251]\nTR a[56:57],v5 offset:1536\nTR a[58:59],v5 offset:5632\n"
"MM v[252:255],a[60:63],v[28:31],v[252:255]\nTR a[60:61],v6 offset:1536\nTR a[62:63],v6 offset:5632\nMM a[116:119],a[124:127],v[28:31],a[116:119]\n"
"s_sub_u32 s90,s90,1\ns_cmp_lg_u32 s90,0\ns_cbranch_scc1 L_top_%=\nMM v[20:23],a[0:3],a[176:179],v[44:47]\nv_exp_f32 v12,v12\n"
"MM v[20:23],a[4:7],a[180:183],v[20:23]\nMM v[20:23],a[8:11],a[184:187],v[20:23]\nv_exp_f32 v13,v13\nMM v[20:23],a[12:15],a[188:191],v[20:23]\n"
"CV v28,v12,v13\nMM v[24:27],a[28:31],a[188:191],v[44:47]\nv_exp_f32 v14,v14\nMM v[24:27],a[24:27],a[184:187],v[24:27]\n"
"MM v[24:27],a[20:23],a[180:183],v[24:27]\nv_exp_f32 v15,v15\nMM v[24:27],a[16:19],a[176:179],v[24:27]\nCV v29,v14,v15\ns_waitcnt lgkmcnt(0)\n"
"MM v[64:67],a[32:35],v[32:35],v[64:67]\nv_exp_f32 v16,v16\nMM v[68:71],a[36:39],v[32:35],v[68:71]\nMM v[72:75],a[40:43],v[32:35],v[72:75]\n"
"v_exp_f32 v17,v17\nMM v[76:79],a[44:47],v[32:35],v[76:79]\nCV v30,v16,v17\nbuffer_load_dwordx4 a[128:131],v7,s[40:43],s73 offen offset:0\n"
"MM v[80:83],a[48:51],v[32:35],v[80:83]\nv_exp_f32 v18,v18\nMM v[84:87],a[52:55],v[32:35],v[84:87]\ns_xor_b32 s91,s64,s65\ns_or_b32 s91,s91,8192\n"
"MM v[88:91],a[56:59],v[32:35],v[88:91]\nv_exp_f32 v19,v19\nMM v[92:95],a[60:63],v[32:35],v[92:95]\nCV v31,v18,v19\nv_xor_b32 v1,s91,v1\n"
"MM a[96:99],a[124:127],v[32:35],a[96:99]\nv_xor_b32 v2,s91,v2\nv_xor_b32 v3,s91,v3\nMM v[12:15],a[0:3],a[192:195],v[48:51]\nv_exp_f32 v20,v20\n"
"MM v[12:15],a[4:7],a[196:199],v[12:15]\nv_xor_b32 v4,s91,v4\nbuffer_load_dwordx4 a[128:131],v7,s[40:43],s73 offen offset:1024\n"
"MM v[12:15],a[8:11],a[200:203],v[12:15]\nv_exp_f32 v21,v21\nMM v[12:15],a[12:15],a[204:207],v[12:15]\nCV v32,v20,v21\n"
"buffer_load_dwordx4 a[128:131],v7,s[40:43],s73 offen offset:2048\nMM v[16:19],a[28:31],a[204:207],v[48:51]\nv_exp_f32 v22,v22\n"
"MM v[16:19],a[24:27],a[200:203],v[16:19]\nbuffer_load_dwordx4 a[128:131],v7,s[40:43],s73 offen offset:3072\nMM v[16:19],a[20:23],a[196:199],v[16:19]\n"
"v_exp_f32 v23,v23\nMM v[16:19],a[16:19],a[192:195],v[16:19]\nCV v33,v22,v23\ns_add_u32 s73,s73,4096\nMM v[96:99],a[32:35],v[28:31],v[96:99]\n"
"v_exp_f32 v24,v24\nMM v[100:103],a[36:39],v[28:31],v[100:103]\nbuffer_load_dwordx4 a[128:131],v7,s[40:43],s73 offen offset:0\n"
"MM v[104:107],a[40:43],v[28:31],v[104:107]\nv_exp_f32 v25,v25\nMM v[108:111],a[44:47],v[28:31],v[108:111]\nCV v34,v24,v25\n"
"buffer_load_dwordx4 a[128:131],v7,s[40:43],s73 offen offset:1024\nMM v[112:115],a[48:51],v[28:31],v[112:115]\nv_exp_f32 v26,v26\n"
"v_mbcnt_lo_u32_b32 v10,-1,0\nv_mbcnt_hi_u32_b32 v10,-1,v10\nv_lshlrev_b32 v7,4,v10\nMM v[116:119],a[52:55],v[28:31],v[116:119]\nv_add_u32 v8,4096,v7\n"
"s_mov_b64 s[56:57],%[ws]\ns_add_u32 s56,s56,%[oo]\ns_addc_u32 s57,s57,0\ns_and_b32 s57,s57,0xffff\nMM v[120:123],a[56:59],v[28:31],v[120:123]\n"
"v_exp_f32 v27,v27\ns_mov_b32 s58,13631488\ns_mov_b32 s59,0x20000\ns_mul_i32 s80,s84,6144\nMM v[124:127],a[60:63],v[28:31],v[124:127]\nCV v35,v26,v27\n"
"s_add_u32 s67,s80,s78\ns_add_u32 s68,s67,106496\ns_add_u32 s69,s67,212992\ns_add_u32 s70,s67,319488\nMM a[100:103],a[124:127],v[28:31],a[100:103]\n"
"s_add_u32 s80,s63,32768\nv_add_u32 v11,s80,v7\ns_lshl_b32 s81,s84,12\nv_add_u32 v0,s81,v11\ns_add_u32 s80,s92,65536\n"
"MM v[20:23],a[0:3],a[208:211],v[52:55]\nv_exp_f32 v12,v12\nv_lshl_add_u32 v10,v10,2,s80\ns_mul_i32 s71,s84,212992\ns_add_u32 s71,s71,s78\n"
"MM v[20:23],a[4:7],a[212:215],v[20:23]\ns_add_u32 s71,s71,12288\ns_add_u32 s72,s71,106496\nMM v[20:23],a[8:11],a[216:219],v[20:23]\nv_exp_f32 v13,v13\n"
"MM v[20:23],a[12:15],a[220:223],v[20:23]\nCV v28,v12,v13\nMM v[24:27],a[28:31],a[220:223],v[52:55]\nv_exp_f32 v14,v14\n"
"MM v[24:27],a[24:27],a[216:219],v[24:27]\nMM v[24:27],a[20:23],a[212:215],v[24:27]\nv_exp_f32 v15,v15\nMM v[24:27],a[16:19],a[208:211],v[24:27]\n"
"CV v29,v14,v15\nMM v[128:131],a[32:35],v[32:35],v[128:131]\nv_exp_f32 v16,v16\nMM v[132:135],a[36:39],v[32:35],v[132:135]\n"
"MM v[136:139],a[40:43],v[32:35],v[136:139]\nv_exp_f32 v17,v17\nMM v[140:143],a[44:47],v[32:35],v[140:143]\nCV v30,v16,v17\n"
"MM v[144:147],a[48:51],v[32:35],v[144:147]\nv_exp_f32 v18,v18\nMM v[148:151],a[52:55],v[32:35],v[148:151]\nMM v[152:155],a[56:59],v[32:35],v[152:155]\n"
"v_exp_f32 v19,v19\nMM v[156:159],a[60:63],v[32:35],v[156:159]\nCV v31,v18,v19\nMM a[104:107],a[124:127],v[32:35],a[104:107]\ns_waitcnt vmcnt(14)\n"
"MM v[12:15],a[0:3],a[224:227],v[56:59]\nv_exp_f32 v20,v20\nMM v[12:15],a[4:7],a[228:231],v[12:15]\nds_read_b128 a[0:3],v1 offset:0\n"
"ds_read_b128 a[4:7],v2 offset:0\nMM v[12:15],a[8:11],a[232:235],v[12:15]\nv_exp_f32 v21,v21\nMM v[12:15],a[12:15],a[236:239],v[12:15]\nCV v32,v20,v21\n"
"ds_read_b128 a[8:11],v3 offset:0\nMM v[16:19],a[28:31],a[236:239],v[56:59]\nv_exp_f32 v22,v22\nMM v[16:19],a[24:27],a[232:235],v[16:19]\n"
"ds_read_b128 a[12:15],v4 offset:0\nds_read_b128 a[28:31],v4 offset:4096\nMM v[16:19],a[20:23],a[228:231],v[16:19]\nv_exp_f32 v23,v23\n"
"MM v[16:19],a[16:19],a[224:227],v[16:19]\nCV v33,v22,v23\nds_read_b128 a[24:27],v3 offset:4096\nMM v[160:163],a[32:35],v[28:31],v[160:163]\n"
"v_exp_f32 v24,v24\nMM v[164:167],a[36:39],v[28:31],v[164:167]\nds_read_b128 a[20:23],v2 offset:4096\nds_read_b128 a[16:19],v1 offset:4096\n"
"MM v[168:171],a[40:43],v[28:31],v[168:171]\nv_exp_f32 v25,v25\nMM v[172:175],a[44:47],v[28:31],v[172:175]\nCV v34,v24,v25\n"
"MM v[176:179],a[48:51],v[28:31],v[176:179]\nv_exp_f32 v26,v26\nMM v[180:183],a[52:55],v[28:31],v[180:183]\nMM v[184:187],a[56:59],v[28:31],v[184:187]\n"
"v_exp_f32 v27,v27\nMM v[188:191],a[60:63],v[28:31],v[188:191]\nCV v35,v26,v27\nMM a[108:111],a[124:127],v[28:31],a[108:111]\ns_waitcnt lgkmcnt(0)\n"
"MM v[20:23],a[0:3],a[240:243],v[60:63]\nv_exp_f32 v12,v12\nMM v[20:23],a[4:7],a[244:247],v[20:23]\nMM v[20:23],a[8:11],a[248:251],v[20:23]\n"
"v_exp_f32 v13,v13\nMM v[20:23],a[12:15],a[252:255],v[20:23]\nCV v28,v12,v13\nMM v[24:27],a[28:31],a[252:255],v[60:63]\nv_exp_f32 v14,v14\n"
"MM v[24:27],a[24:27],a[248:251],v[24:27]\nMM v[24:27],a[20:23],a[244:247],v[24:27]\nv_exp_f32 v15,v15\nMM v[24:27],a[16:19],a[240:243],v[24:27]\n"
"CV v29,v14,v15\nMM v[192:195],a[32:35],v[32:35],v[192:195]\nv_exp_f32 v16,v16\nMM v[196:199],a[36:39],v[32:35],v[196:199]\nv_exp_f32 v20,v20\n"
"MM v[200:203],a[40:43],v[32:35],v[200:203]\nv_exp_f32 v17,v17\nMM v[204:207],a[44:47],v[32:35],v[204:207]\nCV v30,v16,v17\nv_xor_b32 v5,172032,v5\n"
"MM v[208:211],a[48:51],v[32:35],v[208:211]\nv_exp_f32 v18,v18\nMM v[212:215],a[52:55],v[32:35],v[212:215]\nv_exp_f32 v21,v21\n"
"MM v[216:219],a[56:59],v[32:35],v[216:219]\nv_exp_f32 v19,v19\nMM v[220:223],a[60:63],v[32:35],v[220:223]\nCV v31,v18,v19\nv_xor_b32 v6,172032,v6\n"
"MM a[112:115],a[124:127],v[32:35],a[112:115]\nv_exp_f32 v22,v22\nMM v[12:15],a[0:3],a[144:147],v[36:39]\nv_exp_f32 v23,v23\n"
"MM v[12:15],a[4:7],a[148:151],v[12:15]\nCV v32,v20,v21\nCV v33,v22,v23\nMM v[12:15],a[8:11],a[152:155],v[12:15]\nv_exp_f32 v24,v24\n"
"MM v[12:15],a[12:15],a[156:159],v[12:15]\nv_exp_f32 v25,v25\nMM v[16:19],a[28:31],a[156:159],v[36:39]\nv_exp_f32 v26,v26\n"
"MM v[16:19],a[24:27],a[152:155],v[16:19]\nv_exp_f32 v27,v27\nMM v[16:19],a[20:23],a[148:151],v[16:19]\nCV v34,v24,v25\nCV v35,v26,v27\n"
"MM v[16:19],a[16:19],a[144:147],v[16:19]\ns_waitcnt vmcnt(6)\nMM v[224:227],a[32:35],v[28:31],v[224:227]\nTR a[32:33],v5 offset:0\n"
"TR a[34:35],v5 offset:4096\nMM v[228:231],a[36:39],v[28:31],v[228:231]\nTR a[36:37],v6 offset:0\nTR a[38:39],v6 offset:4096\n"
"MM v[232:235],a[40:43],v[28:31],v[232:235]\nTR a[40:41],v5 offset:512\nTR a[42:43],v5 offset:4608\nMM v[236:239],a[44:47],v[28:31],v[236:239]\n"
"TR a[44:45],v6 offset:512\nTR a[46:47],v6 offset:4608\nMM v[240:243],a[48:51],v[28:31],v[240:243]\nTR a[48:49],v5 offset:1024\n"
"TR a[50:51],v5 offset:5120\nMM v[244:247],a[52:55],v[28:31],v[244:247]\nTR a[52:53],v6 offset:1024\nTR a[54:55],v6 offset:5120\n"
"MM v[248:251],a[56:59],v[28:31],v[248:251]\nTR a[56:57],v5 offset:1536\nTR a[58:59],v5 offset:5632\nMM v[252:255],a[60:63],v[28:31],v[252:255]\n"
"TR a[60:61],v6 offset:1536\nTR a[62:63],v6 offset:5632\nMM a[116:119],a[124:127],v[28:31],a[116:119]\nMM v[20:23],a[0:3],a[160:163],v[40:43]\n"
"v_exp_f32 v12,v12\nMM v[20:23],a[4:7],a[164:167],v[20:23]\nMM v[20:23],a[8:11],a[168:171],v[20:23]\nv_exp_f32 v13,v13\n"
"MM v[20:23],a[12:15],a[172:175],v[20:23]\nCV v28,v12,v13\nMM v[24:27],a[28:31],a[172:175],v[40:43]\nv_exp_f32 v14,v14\n"
"MM v[24:27],a[24:27],a[168:171],v[24:27]\nv_xor_b32 v1,8192,v1\nv_xor_b32 v2,8192,v2\nMM v[24:27],a[20:23],a[164:167],v[24:27]\nv_exp_f32 v15,v15\n"
"MM v[24:27],a[16:19],a[160:163],v[24:27]\nCV v29,v14,v15\nv_xor_b32 v3,8192,v3\ns_waitcnt lgkmcnt(0)\nMM a[64:67],a[32:35],v[32:35],a[64:67]\n"
"v_exp_f32 v16,v16\nMM a[68:71],a[36:39],v[32:35],a[68:71]\nv_xor_b32 v4,8192,v4\nMM a[72:75],a[40:43],v[32:35],a[72:75]\nv_exp_f32 v17,v17\n"
"MM a[76:79],a[44:47],v[32:35],a[76:79]\nCV v30,v16,v17\nMM a[80:83],a[48:51],v[32:35],a[80:83]\nv_exp_f32 v18,v18\n"
"MM a[84:87],a[52:55],v[32:35],a[84:87]\nMM a[88:91],a[56:59],v[32:35],a[88:91]\nv_exp_f32 v19,v19\nMM a[92:95],a[60:63],v[32:35],a[92:95]\n"
"CV v31,v18,v19\nMM a[120:123],a[124:127],v[32:35],a[120:123]\ns_barrier\nMM v[12:15],a[0:3],a[176:179],v[44:47]\nv_exp_f32 v20,v20\n"
"MM v[12:15],a[4:7],a[180:183],v[12:15]\nds_read_b128 a[0:3],v1 offset:0\nds_read_b128 a[4:7],v2 offset:0\nMM v[12:15],a[8:11],a[184:187],v[12:15]\n"
"v_exp_f32 v21,v21\nMM v[12:15],a[12:15],a[188:191],v[12:15]\nCV v32,v20,v21\nds_read_b128 a[8:11],v3 offset:0\n"
"MM v[16:19],a[28:31],a[188:191],v[44:47]\nv_exp_f32 v22,v22\nds_write_b128 v11,a[64:67] offset:0\nds_write_b128 v11,a[68:71] offset:1024\n"
"ds_write_b128 v11,a[72:75] offset:2048\nMM v[16:19],a[24:27],a[184:187],v[16:19]\nds_read_b128 a[12:15],v4 offset:0\n"
"ds_read_b128 a[28:31],v4 offset:4096\nds_write_b128 v11,a[76:79] offset:3072\nds_write_b128 v11,a[80:83] offset:4096\n"
"ds_write_b128 v11,a[84:87] offset:5120\nMM v[16:19],a[20:23],a[180:183],v[16:19]\nv_exp_f32 v23,v23\nds_write_b128 v11,a[88:91] offset:6144\n"
"ds_write_b128 v11,a[92:95] offset:7168\nds_write_b32 v10,a120 offset:0\nMM v[16:19],a[16:19],a[176:179],v[16:19]\nCV v33,v22,v23\n"
"ds_read_b128 a[24:27],v3 offset:4096\nMM v[64:67],a[32:35],v[28:31],v[64:67]\nv_exp_f32 v24,v24\nMM v[68:71],a[36:39],v[28:31],v[68:71]\n"
"ds_read_b128 a[20:23],v2 offset:4096\nds_read_b128 a[16:19],v1 offset:4096\nMM v[72:75],a[40:43],v[28:31],v[72:75]\nv_exp_f32 v25,v25\n"
"MM v[76:79],a[44:47],v[28:31],v[76:79]\nCV v34,v24,v25\nMM v[80:83],a[48:51],v[28:31],v[80:83]\nv_exp_f32 v26,v26\n"
"MM v[84:87],a[52:55],v[28:31],v[84:87]\nMM v[88:91],a[56:59],v[28:31],v[88:91]\nv_exp_f32 v27,v27\nMM v[92:95],a[60:63],v[28:31],v[92:95]\n"
"CV v35,v26,v27\nMM a[96:99],a[124:127],v[28:31],a[96:99]\ns_waitcnt lgkmcnt(0)\ns_barrier\nMM v[20:23],a[0:3],a[144:147],v[36:39]\nv_exp_f32 v12,v12\n"
"MM v[20:23],a[4:7],a[148:151],v[20:23]\nMM v[20:23],a[8:11],a[152:155],v[20:23]\nv_exp_f32 v13,v13\nMM v[20:23],a[12:15],a[156:159],v[20:23]\n"
"CV v28,v12,v13\nMM v[24:27],a[28:31],a[156:159],v[36:39]\nv_exp_f32 v14,v14\nMM v[24:27],a[24:27],a[152:155],v[24:27]\n"
"MM v[24:27],a[20:23],a[148:151],v[24:27]\nv_exp_f32 v15,v15\nMM v[24:27],a[16:19],a[144:147],v[24:27]\nCV v29,v14,v15\n"
"MM v[96:99],a[32:35],v[32:35],v[96:99]\nv_exp_f32 v16,v16\nMM v[100:103],a[36:39],v[32:35],v[100:103]\nv_exp_f32 v20,v20\n"
"MM v[104:107],a[40:43],v[32:35],v[104:107]\nv_exp_f32 v17,v17\nMM v[108:111],a[44:47],v[32:35],v[108:111]\nCV v30,v16,v17\nv_xor_b32 v5,8192,v5\n"
"MM v[112:115],a[48:51],v[32:35],v[112:115]\nv_exp_f32 v18,v18\nMM v[116:119],a[52:55],v[32:35],v[116:119]\nv_exp_f32 v21,v21\n"
"MM v[120:123],a[56:59],v[32:35],v[120:123]\nv_exp_f32 v19,v19\nMM v[124:127],a[60:63],v[32:35],v[124:127]\nCV v31,v18,v19\nv_xor_b32 v6,8192,v6\n"
"MM a[100:103],a[124:127],v[32:35],a[100:103]\nv_exp_f32 v22,v22\nMM v[12:15],a[0:3],a[160:163],v[40:43]\nv_exp_f32 v23,v23\n"
"MM v[12:15],a[4:7],a[164:167],v[12:15]\nCV v32,v20,v21\nCV v33,v22,v23\nMM v[12:15],a[8:11],a[168:171],v[12:15]\nv_exp_f32 v24,v24\n"
"MM v[12:15],a[12:15],a[172:175],v[12:15]\nv_exp_f32 v25,v25\nMM v[16:19],a[28:31],a[172:175],v[40:43]\nv_exp_f32 v26,v26\n"
"MM v[16:19],a[24:27],a[168:171],v[16:19]\nv_exp_f32 v27,v27\nMM v[16:19],a[20:23],a[164:167],v[16:19]\nCV v34,v24,v25\nCV v35,v26,v27\n"
"MM v[16:19],a[16:19],a[160:163],v[16:19]\nMM v[128:131],a[32:35],v[28:31],v[128:131]\nTR a[32:33],v5 offset:0\nTR a[34:35],v5 offset:4096\n"
"MM v[132:135],a[36:39],v[28:31],v[132:135]\nTR a[36:37],v6 offset:0\nTR a[38:39],v6 offset:4096\nMM v[136:139],a[40:43],v[28:31],v[136:139]\n"
"TR a[40:41],v5 offset:512\nTR a[42:43],v5 offset:4608\nMM v[140:143],a[44:47],v[28:31],v[140:143]\nTR a[44:45],v6 offset:512\n"
"TR a[46:47],v6 offset:4608\nMM v[144:147],a[48:51],v[28:31],v[144:147]\nTR a[48:49],v5 offset:1024\nTR a[50:51],v5 offset:5120\n"
"MM v[148:151],a[52:55],v[28:31],v[148:151]\nTR a[52:53],v6 offset:1024\nTR a[54:55],v6 offset:5120\nMM v[152:155],a[56:59],v[28:31],v[152:155]\n"
"TR a[56:57],v5 offset:1536\nTR a[58:59],v5 offset:5632\nMM v[156:159],a[60:63],v[28:31],v[156:159]\nTR a[60:61],v6 offset:1536\n"
"TR a[62:63],v6 offset:5632\nMM a[104:107],a[124:127],v[28:31],a[104:107]\nMM v[20:23],a[0:3],a[176:179],v[44:47]\nv_exp_f32 v12,v12\n"
"MM v[20:23],a[4:7],a[180:183],v[20:23]\nMM v[20:23],a[8:11],a[184:187],v[20:23]\nv_exp_f32 v13,v13\nMM v[20:23],a[12:15],a[188:191],v[20:23]\n"
"CV v28,v12,v13\nMM v[24:27],a[28:31],a[188:191],v[44:47]\nv_exp_f32 v14,v14\nMM v[24:27],a[24:27],a[184:187],v[24:27]\n"
"MM v[24:27],a[20:23],a[180:183],v[24:27]\nv_exp_f32 v15,v15\nMM v[24:27],a[16:19],a[176:179],v[24:27]\nCV v29,v14,v15\ns_waitcnt lgkmcnt(0)\n"
"MM v[64:67],a[32:35],v[32:35],v[64:67]\nv_exp_f32 v16,v16\nMM v[68:71],a[36:39],v[32:35],v[68:71]\nMM v[72:75],a[40:43],v[32:35],v[72:75]\n"
"v_exp_f32 v17,v17\nMM v[76:79],a[44:47],v[32:35],v[76:79]\nCV v30,v16,v17\nMM v[80:83],a[48:51],v[32:35],v[80:83]\nv_exp_f32 v18,v18\n"
"MM v[84:87],a[52:55],v[32:35],v[84:87]\nMM v[88:91],a[56:59],v[32:35],v[88:91]\nv_exp_f32 v19,v19\nMM v[92:95],a[60:63],v[32:35],v[92:95]\n"
"CV v31,v18,v19\nMM a[96:99],a[124:127],v[32:35],a[96:99]\nMM v[12:15],a[0:3],a[192:195],v[48:51]\nv_exp_f32 v20,v20\n"
"MM v[12:15],a[4:7],a[196:199],v[12:15]\nMM v[12:15],a[8:11],a[200:203],v[12:15]\nv_exp_f32 v21,v21\nMM v[12:15],a[12:15],a[204:207],v[12:15]\n"
"CV v32,v20,v21\nMM v[16:19],a[28:31],a[204:207],v[48:51]\nv_exp_f32 v22,v22\nv_accvgpr_read_b32 v9,a96\nv_rcp_f32 v9,v9\n"
"MM v[16:19],a[24:27],a[200:203],v[16:19]\nv_mul_f32 v64,v9,v64\nv_mul_f32 v65,v9,v65\nv_mul_f32 v66,v9,v66\nv_mul_f32 v67,v9,v67\nv_mul_f32 v68,v9,v68\n"
"MM v[16:19],a[20:23],a[196:199],v[16:19]\nv_exp_f32 v23,v23\nv_mul_f32 v69,v9,v69\nv_mul_f32 v70,v9,v70\nv_mul_f32 v71,v9,v71\n"
"MM v[16:19],a[16:19],a[192:195],v[16:19]\nCV v33,v22,v23\nCV v64,v64,v65\nCV v65,v66,v67\nCV v66,v68,v69\nCV v67,v70,v71\n"
"MM v[96:99],a[32:35],v[28:31],v[96:99]\nv_exp_f32 v24,v24\nbuffer_store_dwordx4 v[64:67],v7,s[56:59],s67 offen offset:0 sc1\nv_mul_f32 v72,v9,v72\n"
"v_mul_f32 v73,v9,v73\nMM v[100:103],a[36:39],v[28:31],v[100:103]\nv_mul_f32 v74,v9,v74\nv_mul_f32 v75,v9,v75\nv_mul_f32 v76,v9,v76\n"
"v_mul_f32 v77,v9,v77\nv_mul_f32 v78,v9,v78\nMM v[104:107],a[40:43],v[28:31],v[104:107]\nv_exp_f32 v25,v25\nv_mul_f32 v79,v9,v79\nCV v72,v72,v73\n"
"CV v73,v74,v75\nMM v[108:111],a[44:47],v[28:31],v[108:111]\nCV v34,v24,v25\nCV v74,v76,v77\nCV v75,v78,v79\n"
"buffer_store_dwordx4 v[72:75],v7,s[56:59],s68 offen offset:0 sc1\nv_mul_f32 v80,v9,v80\nMM v[112:115],a[48:51],v[28:31],v[112:115]\nv_exp_f32 v26,v26\n"
"v_mul_f32 v81,v9,v81\nv_mul_f32 v82,v9,v82\nv_mul_f32 v83,v9,v83\nMM v[116:119],a[52:55],v[28:31],v[116:119]\nv_mul_f32 v84,v9,v84\n"
"v_mul_f32 v85,v9,v85\nv_mul_f32 v86,v9,v86\nv_mul_f32 v87,v9,v87\nCV v80,v80,v81\nMM v[120:123],a[56:59],v[28:31],v[120:123]\nv_exp_f32 v27,v27\n"
"CV v81,v82,v83\nCV v82,v84,v85\nCV v83,v86,v87\nMM v[124:127],a[60:63],v[28:31],v[124:127]\nCV v35,v26,v27\n"
"buffer_store_dwordx4 v[80:83],v7,s[56:59],s69 offen offset:0 sc1\nv_mul_f32 v88,v9,v88\nv_mul_f32 v89,v9,v89\nv_mul_f32 v90,v9,v90\n"
"MM a[100:103],a[124:127],v[28:31],a[100:103]\nv_mul_f32 v91,v9,v91\nv_mul_f32 v92,v9,v92\nv_mul_f32 v93,v9,v93\nv_mul_f32 v94,v9,v94\n"
"v_mul_f32 v95,v9,v95\nMM v[20:23],a[0:3],a[208:211],v[52:55]\nv_exp_f32 v12,v12\nCV v88,v88,v89\nCV v89,v90,v91\nCV v90,v92,v93\n"
"MM v[20:23],a[4:7],a[212:215],v[20:23]\nCV v91,v94,v95\nbuffer_store_dwordx4 v[88:91],v7,s[56:59],s70 offen offset:0 sc1\n"
"MM v[20:23],a[8:11],a[216:219],v[20:23]\nv_exp_f32 v13,v13\nds_read_b128 v[64:67],v0 offset:0\nds_read_b128 v[68:71],v0 offset:1024\n"
"ds_read_b128 v[72:75],v0 offset:2048\nMM v[20:23],a[12:15],a[220:223],v[20:23]\nCV v28,v12,v13\nds_read_b128 v[76:79],v0 offset:3072\n"
"v_xor_b32 v0,8192,v0\nds_read_b128 v[80:83],v0 offset:0\nds_read_b128 v[84:87],v0 offset:1024\nMM v[24:27],a[28:31],a[220:223],v[52:55]\n"
"v_exp_f32 v14,v14\nds_read_b128 v[88:91],v0 offset:2048\nds_read_b128 v[92:95],v0 offset:3072\nv_xor_b32 v0,1024,v10\n"
"MM v[24:27],a[24:27],a[216:219],v[24:27]\nv_xor_b32 v1,8192,v1\nv_xor_b32 v2,8192,v2\nds_read_b32 v11,v0 offset:0\nv_accvgpr_read_b32 v9,a100\n"
"MM v[24:27],a[20:23],a[212:215],v[24:27]\nv_exp_f32 v15,v15\nv_rcp_f32 v9,v9\ns_nop 0\nv_mul_f32 v96,v9,v96\nMM v[24:27],a[16:19],a[208:211],v[24:27]\n"
"CV v29,v14,v15\nv_xor_b32 v3,8192,v3\nv_mul_f32 v97,v9,v97\nv_mul_f32 v98,v9,v98\nv_mul_f32 v99,v9,v99\nMM v[128:131],a[32:35],v[32:35],v[128:131]\n"
"v_exp_f32 v16,v16\nv_mul_f32 v100,v9,v100\nv_mul_f32 v101,v9,v101\nv_mul_f32 v102,v9,v102\nMM v[132:135],a[36:39],v[32:35],v[132:135]\n"
"v_xor_b32 v4,8192,v4\nv_mul_f32 v103,v9,v103\nCV v96,v96,v97\nCV v97,v98,v99\nCV v98,v100,v101\nMM v[136:139],a[40:43],v[32:35],v[136:139]\n"
"v_exp_f32 v17,v17\nCV v99,v102,v103\nbuffer_store_dwordx4 v[96:99],v7,s[56:59],s67 offen offset:1024 sc1\nv_mul_f32 v104,v9,v104\n"
"MM v[140:143],a[44:47],v[32:35],v[140:143]\nCV v30,v16,v17\nv_mul_f32 v105,v9,v105\nv_mul_f32 v106,v9,v106\nv_mul_f32 v107,v9,v107\n"
"v_mul_f32 v108,v9,v108\nMM v[144:147],a[48:51],v[32:35],v[144:147]\nv_exp_f32 v18,v18\nv_mul_f32 v109,v9,v109\nv_mul_f32 v110,v9,v110\n"
"v_mul_f32 v111,v9,v111\nMM v[148:151],a[52:55],v[32:35],v[148:151]\nCV v104,v104,v105\nCV v105,v106,v107\nCV v106,v108,v109\nCV v107,v110,v111\n"
"buffer_store_dwordx4 v[104:107],v7,s[56:59],s68 offen offset:1024 sc1\nMM v[152:155],a[56:59],v[32:35],v[152:155]\nv_exp_f32 v19,v19\n"
"v_mul_f32 v112,v9,v112\nv_mul_f32 v113,v9,v113\nv_mul_f32 v114,v9,v114\nMM v[156:159],a[60:63],v[32:35],v[156:159]\nCV v31,v18,v19\n"
"v_mul_f32 v115,v9,v115\nv_mul_f32 v116,v9,v116\nv_mul_f32 v117,v9,v117\nv_mul_f32 v118,v9,v118\nMM a[104:107],a[124:127],v[32:35],a[104:107]\n"
"v_mul_f32 v119,v9,v119\nCV v112,v112,v113\nCV v113,v114,v115\nCV v114,v116,v117\nCV v115,v118,v119\nMM v[12:15],a[0:3],a[224:227],v[56:59]\n"
"v_exp_f32 v20,v20\nbuffer_store_dwordx4 v[112:115],v7,s[56:59],s69 offen offset:1024 sc1\nv_mul_f32 v120,v9,v120\nv_mul_f32 v121,v9,v121\n"
"MM v[12:15],a[4:7],a[228:231],v[12:15]\nds_read_b128 a[0:3],v1 offset:0\nds_read_b128 a[4:7],v2 offset:0\nv_mul_f32 v122,v9,v122\n"
"v_mul_f32 v123,v9,v123\nv_mul_f32 v124,v9,v124\nMM v[12:15],a[8:11],a[232:235],v[12:15]\nv_exp_f32 v21,v21\nv_mul_f32 v125,v9,v125\n"
"v_mul_f32 v126,v9,v126\nv_mul_f32 v127,v9,v127\nMM v[12:15],a[12:15],a[236:239],v[12:15]\nCV v32,v20,v21\nds_read_b128 a[8:11],v3 offset:0\n"
"CV v120,v120,v121\nCV v121,v122,v123\nCV v122,v124,v125\nMM v[16:19],a[28:31],a[236:239],v[56:59]\nv_exp_f32 v22,v22\nCV v123,v126,v127\n"
"buffer_store_dwordx4 v[120:123],v7,s[56:59],s70 offen offset:1024 sc1\nv_accvgpr_read_b32 v9,a120\nMM v[16:19],a[24:27],a[232:235],v[16:19]\n"
"ds_read_b128 a[12:15],v4 offset:0\nds_read_b128 a[28:31],v4 offset:4096\ns_waitcnt lgkmcnt(5)\nv_add_f32 v9,v9,v11\nv_rcp_f32 v9,v9\n"
"MM v[16:19],a[20:23],a[228:231],v[16:19]\nv_exp_f32 v23,v23\nv_add_f32 v64,v64,v80\nv_add_f32 v65,v65,v81\nv_add_f32 v66,v66,v82\n"
"MM v[16:19],a[16:19],a[224:227],v[16:19]\nCV v33,v22,v23\nds_read_b128 a[24:27],v3 offset:4096\nv_add_f32 v67,v67,v83\nv_add_f32 v68,v68,v84\n"
"v_add_f32 v69,v69,v85\nMM v[160:163],a[32:35],v[28:31],v[160:163]\nv_exp_f32 v24,v24\nv_add_f32 v70,v70,v86\nv_add_f32 v71,v71,v87\n"
"v_add_f32 v72,v72,v88\nMM v[164:167],a[36:39],v[28:31],v[164:167]\nds_read_b128 a[20:23],v2 offset:4096\nds_read_b128 a[16:19],v1 offset:4096\n"
"v_add_f32 v73,v73,v89\nv_add_f32 v74,v74,v90\nv_add_f32 v75,v75,v91\nMM v[168:171],a[40:43],v[28:31],v[168:171]\nv_exp_f32 v25,v25\n"
"v_add_f32 v76,v76,v92\nv_add_f32 v77,v77,v93\nv_add_f32 v78,v78,v94\nMM v[172:175],a[44:47],v[28:31],v[172:175]\nCV v34,v24,v25\nv_add_f32 v79,v79,v95\n"
"v_mul_f32 v64,v9,v64\nv_mul_f32 v65,v9,v65\nv_mul_f32 v66,v9,v66\nMM v[176:179],a[48:51],v[28:31],v[176:179]\nv_exp_f32 v26,v26\nv_mul_f32 v67,v9,v67\n"
"v_mul_f32 v68,v9,v68\nv_mul_f32 v69,v9,v69\nMM v[180:183],a[52:55],v[28:31],v[180:183]\nv_mul_f32 v70,v9,v70\nv_mul_f32 v71,v9,v71\n"
"v_mul_f32 v72,v9,v72\nv_mul_f32 v73,v9,v73\nv_mul_f32 v74,v9,v74\nMM v[184:187],a[56:59],v[28:31],v[184:187]\nv_exp_f32 v27,v27\nv_mul_f32 v75,v9,v75\n"
"v_mul_f32 v76,v9,v76\nv_mul_f32 v77,v9,v77\nMM v[188:191],a[60:63],v[28:31],v[188:191]\nCV v35,v26,v27\nv_mul_f32 v78,v9,v78\nv_mul_f32 v79,v9,v79\n"
"CV v64,v64,v65\nCV v65,v66,v67\nMM a[108:111],a[124:127],v[28:31],a[108:111]\nCV v66,v68,v69\nCV v67,v70,v71\nCV v68,v72,v73\nCV v69,v74,v75\n"
"CV v70,v76,v77\ns_waitcnt lgkmcnt(0)\nMM v[20:23],a[0:3],a[192:195],v[48:51]\nv_exp_f32 v12,v12\nCV v71,v78,v79\n"
"buffer_store_dwordx4 v[64:67],v7,s[56:59],s71 offen offset:0 sc1\nbuffer_store_dwordx4 v[68:71],v7,s[56:59],s72 offen offset:0 sc1\n"
"MM v[20:23],a[4:7],a[196:199],v[20:23]\nv_accvgpr_read_b32 v9,a104\nv_rcp_f32 v9,v9\ns_nop 0\nv_mul_f32 v128,v9,v128\nv_mul_f32 v129,v9,v129\n"
"MM v[20:23],a[8:11],a[200:203],v[20:23]\nv_exp_f32 v13,v13\nv_mul_f32 v130,v9,v130\nv_mul_f32 v131,v9,v131\nv_mul_f32 v132,v9,v132\n"
"MM v[20:23],a[12:15],a[204:207],v[20:23]\nCV v28,v12,v13\nv_mul_f32 v133,v9,v133\nv_mul_f32 v134,v9,v134\nv_mul_f32 v135,v9,v135\nCV v128,v128,v129\n"
"MM v[24:27],a[28:31],a[204:207],v[48:51]\nv_exp_f32 v14,v14\nCV v129,v130,v131\nCV v130,v132,v133\nCV v131,v134,v135\n"
"MM v[24:27],a[24:27],a[200:203],v[24:27]\nbuffer_store_dwordx4 v[128:131],v7,s[56:59],s67 offen offset:2048 sc1\nv_mul_f32 v136,v9,v136\n"
"v_mul_f32 v137,v9,v137\nv_mul_f32 v138,v9,v138\nv_mul_f32 v139,v9,v139\nMM v[24:27],a[20:23],a[196:199],v[24:27]\nv_exp_f32 v15,v15\n"
"v_mul_f32 v140,v9,v140\nv_mul_f32 v141,v9,v141\nv_mul_f32 v142,v9,v142\nMM v[24:27],a[16:19],a[192:195],v[24:27]\nCV v29,v14,v15\n"
"v_mul_f32 v143,v9,v143\nCV v136,v136,v137\nCV v137,v138,v139\nCV v138,v140,v141\nMM v[192:195],a[32:35],v[32:35],v[192:195]\nv_exp_f32 v16,v16\n"
"CV v139,v142,v143\nbuffer_store_dwordx4 v[136:139],v7,s[56:59],s68 offen offset:2048 sc1\nv_mul_f32 v144,v9,v144\n"
"MM v[196:199],a[36:39],v[32:35],v[196:199]\nv_exp_f32 v20,v20\nv_mul_f32 v145,v9,v145\nv_mul_f32 v146,v9,v146\nv_mul_f32 v147,v9,v147\n"
"MM v[200:203],a[40:43],v[32:35],v[200:203]\nv_exp_f32 v17,v17\nv_mul_f32 v148,v9,v148\nv_mul_f32 v149,v9,v149\nv_mul_f32 v150,v9,v150\n"
"MM v[204:207],a[44:47],v[32:35],v[204:207]\nCV v30,v16,v17\nv_xor_b32 v5,8192,v5\nv_mul_f32 v151,v9,v151\nCV v144,v144,v145\nCV v145,v146,v147\n"
"MM v[208:211],a[48:51],v[32:35],v[208:211]\nv_exp_f32 v18,v18\nCV v146,v148,v149\nCV v147,v150,v151\n"
"buffer_store_dwordx4 v[144:147],v7,s[56:59],s69 offen offset:2048 sc1\nMM v[212:215],a[52:55],v[32:35],v[212:215]\nv_exp_f32 v21,v21\n"
"v_mul_f32 v152,v9,v152\nv_mul_f32 v153,v9,v153\nv_mul_f32 v154,v9,v154\nMM v[216:219],a[56:59],v[32:35],v[216:219]\nv_exp_f32 v19,v19\n"
"v_mul_f32 v155,v9,v155\nv_mul_f32 v156,v9,v156\nv_mul_f32 v157,v9,v157\nMM v[220:223],a[60:63],v[32:35],v[220:223]\nCV v31,v18,v19\n"
"v_xor_b32 v6,8192,v6\nv_mul_f32 v158,v9,v158\nv_mul_f32 v159,v9,v159\nCV v152,v152,v153\nMM a[112:115],a[124:127],v[32:35],a[112:115]\n"
"v_exp_f32 v22,v22\nCV v153,v154,v155\nCV v154,v156,v157\nCV v155,v158,v159\nMM v[12:15],a[0:3],a[208:211],v[52:55]\nv_exp_f32 v23,v23\n"
"buffer_store_dwordx4 v[152:155],v7,s[56:59],s70 offen offset:2048 sc1\nMM v[12:15],a[4:7],a[212:215],v[12:15]\nCV v32,v20,v21\nCV v33,v22,v23\n"
"MM v[12:15],a[8:11],a[216:219],v[12:15]\nv_exp_f32 v24,v24\nMM v[12:15],a[12:15],a[220:223],v[12:15]\nv_exp_f32 v25,v25\n"
"MM v[16:19],a[28:31],a[220:223],v[52:55]\nv_exp_f32 v26,v26\nMM v[16:19],a[24:27],a[216:219],v[16:19]\nv_exp_f32 v27,v27\n"
"MM v[16:19],a[20:23],a[212:215],v[16:19]\nCV v34,v24,v25\nCV v35,v26,v27\nMM v[16:19],a[16:19],a[208:211],v[16:19]\n"
"MM v[224:227],a[32:35],v[28:31],v[224:227]\nTR a[32:33],v5 offset:0\nTR a[34:35],v5 offset:4096\nMM v[228:231],a[36:39],v[28:31],v[228:231]\n"
"TR a[36:37],v6 offset:0\nTR a[38:39],v6 offset:4096\nMM v[232:235],a[40:43],v[28:31],v[232:235]\nTR a[40:41],v5 offset:512\nTR a[42:43],v5 offset:4608\n"
"MM v[236:239],a[44:47],v[28:31],v[236:239]\nTR a[44:45],v6 offset:512\nTR a[46:47],v6 offset:4608\nMM v[240:243],a[48:51],v[28:31],v[240:243]\n"
"TR a[48:49],v5 offset:1024\nTR a[50:51],v5 offset:5120\nMM v[244:247],a[52:55],v[28:31],v[244:247]\nTR a[52:53],v6 offset:1024\n"
"TR a[54:55],v6 offset:5120\nMM v[248:251],a[56:59],v[28:31],v[248:251]\nTR a[56:57],v5 offset:1536\nTR a[58:59],v5 offset:5632\n"
"MM v[252:255],a[60:63],v[28:31],v[252:255]\nTR a[60:61],v6 offset:1536\nTR a[62:63],v6 offset:5632\nMM a[116:119],a[124:127],v[28:31],a[116:119]\n"
"MM v[20:23],a[0:3],a[224:227],v[56:59]\nv_exp_f32 v12,v12\nMM v[20:23],a[4:7],a[228:231],v[20:23]\nMM v[20:23],a[8:11],a[232:235],v[20:23]\n"
"v_exp_f32 v13,v13\nMM v[20:23],a[12:15],a[236:239],v[20:23]\nCV v28,v12,v13\nMM v[24:27],a[28:31],a[236:239],v[56:59]\nv_exp_f32 v14,v14\n"
"MM v[24:27],a[24:27],a[232:235],v[24:27]\nMM v[24:27],a[20:23],a[228:231],v[24:27]\nv_exp_f32 v15,v15\nMM v[24:27],a[16:19],a[224:227],v[24:27]\n"
"CV v29,v14,v15\ns_waitcnt lgkmcnt(0)\nMM v[160:163],a[32:35],v[32:35],v[160:163]\nv_exp_f32 v16,v16\nMM v[164:167],a[36:39],v[32:35],v[164:167]\n"
"MM v[168:171],a[40:43],v[32:35],v[168:171]\nv_exp_f32 v17,v17\nMM v[172:175],a[44:47],v[32:35],v[172:175]\nCV v30,v16,v17\n"
"MM v[176:179],a[48:51],v[32:35],v[176:179]\nv_exp_f32 v18,v18\nMM v[180:183],a[52:55],v[32:35],v[180:183]\nMM v[184:187],a[56:59],v[32:35],v[184:187]\n"
"v_exp_f32 v19,v19\nMM v[188:191],a[60:63],v[32:35],v[188:191]\nCV v31,v18,v19\nMM a[108:111],a[124:127],v[32:35],a[108:111]\ns_nop 0\n"
"MM v[192:195],a[32:35],v[28:31],v[192:195]\nv_exp_f32 v20,v20\nMM v[196:199],a[36:39],v[28:31],v[196:199]\nv_exp_f32 v21,v21\n"
"MM v[200:203],a[40:43],v[28:31],v[200:203]\nv_exp_f32 v22,v22\nCV v32,v20,v21\nMM v[204:207],a[44:47],v[28:31],v[204:207]\nv_exp_f32 v23,v23\n"
"MM v[208:211],a[48:51],v[28:31],v[208:211]\nv_exp_f32 v24,v24\nCV v33,v22,v23\nv_accvgpr_read_b32 v9,a108\nMM v[212:215],a[52:55],v[28:31],v[212:215]\n"
"v_exp_f32 v25,v25\nv_rcp_f32 v9,v9\ns_nop 0\nv_mul_f32 v160,v9,v160\nMM v[216:219],a[56:59],v[28:31],v[216:219]\nv_exp_f32 v26,v26\nCV v34,v24,v25\n"
"v_mul_f32 v161,v9,v161\nv_mul_f32 v162,v9,v162\nMM v[220:223],a[60:63],v[28:31],v[220:223]\nv_exp_f32 v27,v27\nv_mul_f32 v163,v9,v163\n"
"v_mul_f32 v164,v9,v164\nv_mul_f32 v165,v9,v165\nMM a[112:115],a[124:127],v[28:31],a[112:115]\nCV v35,v26,v27\nv_mul_f32 v166,v9,v166\n"
"v_mul_f32 v167,v9,v167\nCV v160,v160,v161\nCV v161,v162,v163\nMM v[224:227],a[32:35],v[32:35],v[224:227]\nCV v162,v164,v165\nCV v163,v166,v167\n"
"buffer_store_dwordx4 v[160:163],v7,s[56:59],s67 offen offset:3072 sc1\nv_mul_f32 v168,v9,v168\nv_mul_f32 v169,v9,v169\n"
"MM v[228:231],a[36:39],v[32:35],v[228:231]\nv_mul_f32 v170,v9,v170\nv_mul_f32 v171,v9,v171\nv_mul_f32 v172,v9,v172\nv_mul_f32 v173,v9,v173\n"
"v_mul_f32 v174,v9,v174\nMM v[232:235],a[40:43],v[32:35],v[232:235]\nv_mul_f32 v175,v9,v175\nCV v168,v168,v169\nCV v169,v170,v171\nCV v170,v172,v173\n"
"CV v171,v174,v175\nMM v[236:239],a[44:47],v[32:35],v[236:239]\nbuffer_store_dwordx4 v[168:171],v7,s[56:59],s68 offen offset:3072 sc1\n"
"v_mul_f32 v176,v9,v176\nv_mul_f32 v177,v9,v177\nv_mul_f32 v178,v9,v178\nv_mul_f32 v179,v9,v179\nMM v[240:243],a[48:51],v[32:35],v[240:243]\n"
"v_mul_f32 v180,v9,v180\nv_mul_f32 v181,v9,v181\nv_mul_f32 v182,v9,v182\nv_mul_f32 v183,v9,v183\nCV v176,v176,v177\n"
"MM v[244:247],a[52:55],v[32:35],v[244:247]\nCV v177,v178,v179\nCV v178,v180,v181\nCV v179,v182,v183\n"
"buffer_store_dwordx4 v[176:179],v7,s[56:59],s69 offen offset:3072 sc1\nv_mul_f32 v184,v9,v184\nMM v[248:251],a[56:59],v[32:35],v[248:251]\n"
"v_mul_f32 v185,v9,v185\nv_mul_f32 v186,v9,v186\nv_mul_f32 v187,v9,v187\nv_mul_f32 v188,v9,v188\nv_mul_f32 v189,v9,v189\n"
"MM v[252:255],a[60:63],v[32:35],v[252:255]\nv_mul_f32 v190,v9,v190\nv_mul_f32 v191,v9,v191\nCV v184,v184,v185\nCV v185,v186,v187\nCV v186,v188,v189\n"
"MM a[116:119],a[124:127],v[32:35],a[116:119]\nCV v187,v190,v191\nbuffer_store_dwordx4 v[184:187],v7,s[56:59],s70 offen offset:3072 sc1\n"
"v_accvgpr_read_b32 v9,a112\nv_rcp_f32 v9,v9\ns_nop 0\nv_mul_f32 v192,v9,v192\nv_mul_f32 v193,v9,v193\nv_mul_f32 v194,v9,v194\nv_mul_f32 v195,v9,v195\n"
"v_mul_f32 v196,v9,v196\nv_mul_f32 v197,v9,v197\nv_mul_f32 v198,v9,v198\nv_mul_f32 v199,v9,v199\nCV v192,v192,v193\nCV v193,v194,v195\n"
"CV v194,v196,v197\nCV v195,v198,v199\nbuffer_store_dwordx4 v[192:195],v8,s[56:59],s67 offen offset:0 sc1\n"
"v_pk_mul_f32 v[200:201],v[200:201],v[8:9] op_sel:[0,1]\nv_pk_mul_f32 v[202:203],v[202:203],v[8:9] op_sel:[0,1]\n"
"v_pk_mul_f32 v[204:205],v[204:205],v[8:9] op_sel:[0,1]\nv_pk_mul_f32 v[206:207],v[206:207],v[8:9] op_sel:[0,1]\nCV v200,v200,v201\nCV v201,v202,v203\n"
"CV v202,v204,v205\nCV v203,v206,v207\nbuffer_store_dwordx4 v[200:203],v8,s[56:59],s68 offen offset:0 sc1\n"
"v_pk_mul_f32 v[208:209],v[208:209],v[8:9] op_sel:[0,1]\nv_pk_mul_f32 v[210:211],v[210:211],v[8:9] op_sel:[0,1]\n"
"v_pk_mul_f32 v[212:213],v[212:213],v[8:9] op_sel:[0,1]\nv_pk_mul_f32 v[214:215],v[214:215],v[8:9] op_sel:[0,1]\nCV v208,v208,v209\nCV v209,v210,v211\n"
"CV v210,v212,v213\nCV v211,v214,v215\nbuffer_store_dwordx4 v[208:211],v8,s[56:59],s69 offen offset:0 sc1\n"
"v_pk_mul_f32 v[216:217],v[216:217],v[8:9] op_sel:[0,1]\nv_pk_mul_f32 v[218:219],v[218:219],v[8:9] op_sel:[0,1]\n"
"v_pk_mul_f32 v[220:221],v[220:221],v[8:9] op_sel:[0,1]\nv_pk_mul_f32 v[222:223],v[222:223],v[8:9] op_sel:[0,1]\nCV v216,v216,v217\nCV v217,v218,v219\n"
"CV v218,v220,v221\nCV v219,v222,v223\nbuffer_store_dwordx4 v[216:219],v8,s[56:59],s70 offen offset:0 sc1\nv_accvgpr_read_b32 v9,a116\nv_rcp_f32 v9,v9\n"
"s_nop 0\nv_pk_mul_f32 v[224:225],v[224:225],v[8:9] op_sel:[0,1]\nv_pk_mul_f32 v[226:227],v[226:227],v[8:9] op_sel:[0,1]\n"
"v_pk_mul_f32 v[228:229],v[228:229],v[8:9] op_sel:[0,1]\nv_pk_mul_f32 v[230:231],v[230:231],v[8:9] op_sel:[0,1]\nCV v224,v224,v225\nCV v225,v226,v227\n"
"CV v226,v228,v229\nCV v227,v230,v231\nbuffer_store_dwordx4 v[224:227],v8,s[56:59],s67 offen offset:1024 sc1\n"
"v_pk_mul_f32 v[232:233],v[232:233],v[8:9] op_sel:[0,1]\nv_pk_mul_f32 v[234:235],v[234:235],v[8:9] op_sel:[0,1]\n"
"v_pk_mul_f32 v[236:237],v[236:237],v[8:9] op_sel:[0,1]\nv_pk_mul_f32 v[238:239],v[238:239],v[8:9] op_sel:[0,1]\nCV v232,v232,v233\nCV v233,v234,v235\n"
"CV v234,v236,v237\nCV v235,v238,v239\nbuffer_store_dwordx4 v[232:235],v8,s[56:59],s68 offen offset:1024 sc1\n"
"v_pk_mul_f32 v[240:241],v[240:241],v[8:9] op_sel:[0,1]\nv_pk_mul_f32 v[242:243],v[242:243],v[8:9] op_sel:[0,1]\n"
"v_pk_mul_f32 v[244:245],v[244:245],v[8:9] op_sel:[0,1]\nv_pk_mul_f32 v[246:247],v[246:247],v[8:9] op_sel:[0,1]\nCV v240,v240,v241\nCV v241,v242,v243\n"
"CV v242,v244,v245\nCV v243,v246,v247\nbuffer_store_dwordx4 v[240:243],v8,s[56:59],s69 offen offset:1024 sc1\n"
"v_pk_mul_f32 v[248:249],v[248:249],v[8:9] op_sel:[0,1]\nv_pk_mul_f32 v[250:251],v[250:251],v[8:9] op_sel:[0,1]\n"
"v_pk_mul_f32 v[252:253],v[252:253],v[8:9] op_sel:[0,1]\nv_pk_mul_f32 v[254:255],v[254:255],v[8:9] op_sel:[0,1]\nCV v248,v248,v249\nCV v249,v250,v251\n"
"CV v250,v252,v253\nCV v251,v254,v255\nbuffer_store_dwordx4 v[248:251],v8,s[56:59],s70 offen offset:1024 sc1\nv_accvgpr_read_b32 v12,a96\n"
"v_accvgpr_read_b32 v13,a100\nv_accvgpr_read_b32 v14,a104\nv_accvgpr_read_b32 v15,a108\nv_accvgpr_read_b32 v16,a112\nv_accvgpr_read_b32 v17,a116\n"
"v_accvgpr_read_b32 v18,a120\nv_add_f32 v12,v12,v13\nv_add_f32 v14,v14,v15\nv_add_f32 v16,v16,v17\nv_add_f32 v12,v12,v18\nv_add_f32 v14,v14,v16\n"
"v_add_f32 v12,v12,v14\nv_cmp_ngt_f32 vcc,s79,v12\nv_and_b32 v13,0xfffff3ff,v10\ns_nop 0\ns_or_b32 s76,vcc_lo,vcc_hi\nv_mov_b32 v12,s76\n"
"ds_write_b32 v10,v12 offset:256\ns_waitcnt lgkmcnt(0)\ns_barrier\nds_read_b32 v32,v13 offset:256\nds_read_b32 v33,v13 offset:1280\n"
"ds_read_b32 v34,v13 offset:2304\nds_read_b32 v35,v13 offset:3328\ns_waitcnt lgkmcnt(2)\nv_or_b32 v32,v32,v33\ns_waitcnt lgkmcnt(0)\n"
"v_or3_b32 v32,v32,v34,v35\ns_nop 0\nv_readfirstlane_b32 s80,v32\ns_nop 3\ns_mov_b32 %[flag],s80\ns_cmp_eq_u32 s80,0\ns_cbranch_scc1 L_p1_done_%=\n"
"s_mov_b64 s[82:83],%[ws]\ns_add_u32 s82,s82,%[oqs]\ns_addc_u32 s83,s83,0\nglobal_store_dwordx4 v7,a[144:147],s[82:83] offset:0\n"
"global_store_dwordx4 v7,a[148:151],s[82:83] offset:1024\nglobal_store_dwordx4 v7,a[152:155],s[82:83] offset:2048\n"
"global_store_dwordx4 v7,a[156:159],s[82:83] offset:3072\nv_add_u32 v7,4096,v7\nglobal_store_dwordx4 v7,a[160:163],s[82:83] offset:0\n"
"global_store_dwordx4 v7,a[164:167],s[82:83] offset:1024\nglobal_store_dwordx4 v7,a[168:171],s[82:83] offset:2048\n"
"global_store_dwordx4 v7,a[172:175],s[82:83] offset:3072\nv_add_u32 v7,4096,v7\nglobal_store_dwordx4 v7,a[176:179],s[82:83] offset:0\n"
"global_store_dwordx4 v7,a[180:183],s[82:83] offset:1024\nglobal_store_dwordx4 v7,a[184:187],s[82:83] offset:2048\n"
"global_store_dwordx4 v7,a[188:191],s[82:83] offset:3072\nv_add_u32 v7,4096,v7\nglobal_store_dwordx4 v7,a[192:195],s[82:83] offset:0\n"
"global_store_dwordx4 v7,a[196:199],s[82:83] offset:1024\nglobal_store_dwordx4 v7,a[200:203],s[82:83] offset:2048\n"
"global_store_dwordx4 v7,a[204:207],s[82:83] offset:3072\nv_add_u32 v7,4096,v7\nglobal_store_dwordx4 v7,a[208:211],s[82:83] offset:0\n"
"global_store_dwordx4 v7,a[212:215],s[82:83] offset:1024\nglobal_store_dwordx4 v7,a[216:219],s[82:83] offset:2048\n"
"global_store_dwordx4 v7,a[220:223],s[82:83] offset:3072\nv_add_u32 v7,4096,v7\nglobal_store_dwordx4 v7,a[224:227],s[82:83] offset:0\n"
"global_store_dwordx4 v7,a[228:231],s[82:83] offset:1024\nglobal_store_dwordx4 v7,a[232:235],s[82:83] offset:2048\n"
"global_store_dwordx4 v7,a[236:239],s[82:83] offset:3072\nv_add_u32 v7,4096,v7\nglobal_store_dwordx4 v7,a[240:243],s[82:83] offset:0\n"
"global_store_dwordx4 v7,a[244:247],s[82:83] offset:1024\nglobal_store_dwordx4 v7,a[248:251],s[82:83] offset:2048\n"
"global_store_dwordx4 v7,a[252:255],s[82:83] offset:3072\nv_add_u32 v7,4096,v7\ns_waitcnt vmcnt(0)\ns_barrier\nL_p1_done_%=:\n"
".purgem MM\n"
".purgem CV\n"
".purgem TR\n"
      : [flag] "=&s"(flag), [ep] "=&s"(epoch), [ws] "=&s"(wsp), [wo] "=&s"(wop), [out] "=&s"(outp)
      : [ka] "s"(ka), [ko] "s"(c.br * 40u), [ox] "s"(c.b * (SQ * DM * 2u)), [oq] "s"(c.j * 512u), [okv] "s"((c.b * NH + c.h) * (SK * DH * 2u)), [oo] "s"(c.br * OBR_BYTES), [w] "s"(w),
        [rot] "s"(((bid * 2u + (w >> 1)) * 2654435761u) >> 26), [ooff] "s"((c.h * 416u + c.b * 13u) * 1024u), [krot] "s"((c.b & 1u) + 2u * (c.j & 1u)),
        [oqs] "s"((unsigned)WS_QS + (bid * 4u + w) * 28672u), [owt] "s"((c.j >> 3) * 2621440u + (((c.j & 7u) * 8u + c.b) * 4u + w) * (WT_N * 1024u))
      : "v0","v1","v2","v3","v4","v5","v6","v7","v8","v9","v10","v11","v12","v13","v14","v15","v16","v17","v18","v19","v20","v21","v22","v23","v24","v25","v26","v27","v28","v29","v30","v31","v32","v33","v34","v35","v36","v37","v38","v39","v40","v41","v42","v43","v44","v45","v46","v47","v48","v49","v50","v51","v52","v53","v54","v55","v56","v57","v58","v59","v60","v61","v62","v63","v64","v65","v66","v67","v68","v69","v70","v71","v72","v73","v74","v75","v76","v77","v78","v79","v80","v81","v82","v83","v84","v85","v86","v87","v88","v89","v90","v91","v92","v93","v94","v95","v96","v97","v98","v99","v100","v101","v102","v103","v104","v105","v106","v107","v108","v109","v110","v111","v112","v113","v114","v115","v116","v117","v118","v119","v120","v121","v122","v123","v124","v125","v126","v127","v128","v129","v130","v131","v132","v133","v134","v135","v136","v137","v138","v139","v140","v141","v142","v143","v144","v145","v146","v147","v148","v149","v150","v151","v152","v153","v154","v155","v156","v157","v158","v159","v160","v161","v162","v163","v164","v165","v166","v167","v168","v169","v170","v171","v172","v173","v174","v175","v176","v177","v178","v179","v180","v181","v182","v183","v184","v185","v186","v187","v188","v189","v190","v191","v192","v193","v194","v195","v196","v197","v198","v199","v200","v201","v202","v203","v204","v205","v206","v207","v208","v209","v210","v211","v212","v213","v214","v215","v216","v217","v218","v219","v220","v221","v222","v223","v224","v225","v226","v227","v228","v229","v230","v231","v232","v233","v234","v235","v236","v237","v238","v239","v240","v241","v242","v243","v244","v245","v246","v247","v248","v249","v250","v251","v252","v253","v254","v255","a0","a1","a2","a3","a4","a5","a6","a7","a8","a9","a10","a11","a12","a13","a14","a15","a16","a17","a18","a19","a20","a21","a22","a23","a24","a25","a26","a27","a28","a29","a30","a31","a32","a33","a34","a35","a36","a37","a38","a39","a40","a41","a42","a43","a44","a45","a46","a47","a48","a49","a50","a51","a52","a53","a54","a55","a56","a57","a58","a59","a60","a61","a62","a63","a64","a65","a66","a67","a68","a69","a70","a71","a72","a73","a74","a75","a76","a77","a78","a79","a80","a81","a82","a83","a84","a85","a86","a87","a88","a89","a90","a91","a92","a93","a94","a95","a96","a97","a98","a99","a100","a101","a102","a103","a104","a105","a106","a107","a108","a109","a110","a111","a112","a113","a114","a115","a116","a117","a118","a119","a120","a121","a122","a123","a124","a125","a126","a127","a128","a129","a130","a131","a132","a133","a134","a135","a136","a137","a138","a139","a140","a141","a142","a143","a144","a145","a146","a147","a148","a149","a150","a151","a152","a153","a154","a155","a156","a157","a158","a159","a160","a161","a162","a163","a164","a165","a166","a167","a168","a169","a170","a171","a172","a173","a174","a175","a176","a177","a178","a179","a180","a181","a182","a183","a184","a185","a186","a187","a188","a189","a190","a191","a192","a193","a194","a195","a196","a197","a198","a199","a200","a201","a202","a203","a204","a205","a206","a207","a208","a209","a210","a211","a212","a213","a214","a215","a216","a217","a218","a219","a220","a221","a222","a223","a224","a225","a226","a227","a228","a229","a230","a231","a232","a233","a234","a235","a236","a237","a238","a239","a240","a241","a242","a243","a244","a245","a246","a247","a248","a249","a250","a251","a252","a253","a254","a255","s40","s41","s42","s43","s44","s45","s46","s47","s48","s49","s50","s51","s52","s53","s54","s55","s56","s57","s58","s59","s60","s61","s62","s63","s64","s65","s66","s67","s68","s69","s70","s71","s72","s73","s74","s75","s76","s77","s78","s79","s80","s81","s82","s83","s84","s85","s86","s87","s88","s89","s90","s91","s92","s93","vcc","scc","memory");
  }
  asm volatile("" : "+s"(ka), "+s"(bid), "+s"(w));
  const Geo c(bid, w);
  kargs_t kb = ka + 5u * c.br;
  GAS char* Of = (GAS char*)wsp;
  GAS char* Ob = Of + (size_t)c.br * OBR_BYTES;
  GAS u64* flags = (GAS u64*)(Of + WS_FLAGS);
  const u64 tagu = epoch * 6364136223846793005ull + 1442695040888963407ull;
  const unsigned br = c.br, b = c.b, j = c.j, h = c.h, ng = c.ng, rb0 = c.rb0, nrb = c.nrb, kh = c.kh, kp = w >> 1, nh = w & 1u;
  if (__builtin_expect(flag != 0, 0)) {
    const unsigned lane = __builtin_amdgcn_mbcnt_hi(~0u, __builtin_amdgcn_mbcnt_lo(~0u, 0u)), rr = lane & 15u, G = lane >> 4, g = w & 1u;
    const GAS u16* K = (const GAS u16*)kb[1] + (size_t)(b * NH + h) * SK * DH;
    const GAS u16* V = (const GAS u16*)kb[2] + (size_t)(b * NH + h) * SK * DH;
    const GAS char* Qs = Of + WS_QS + (bid * 4u + w) * 28672u;
#pragma unroll 1
    for (unsigned blk = 0; blk < 7u - g; ++blk) {
      bf16x8 q[4];
#pragma unroll
      for (int e = 0; e < 4; ++e) q[e] = *(const GAS bf16x8*)(Qs + (blk * 4u + (e ^ (2u * g))) * 1024u + lane * 16u);
      f32x4 o[8];
#pragma unroll
      for (int c = 0; c < 8; ++c) o[c] = f32x4{0.f, 0.f, 0.f, 0.f};
      float m = -__builtin_inff(), l = 0.f;
#pragma unroll 1
      for (unsigned k0 = 0; k0 < SK; k0 += 32) {
        f32x4 s0 = {0.f, 0.f, 0.f, 0.f}, s1 = s0;
#pragma unroll
        for (int e = 0; e < 4; ++e) {
          s0 = MFMA16(*(const GAS bf16x8*)(K + (k0 + rr) * DH + 32 * e + 8 * G), q[e], s0);
          s1 = MFMA16(*(const GAS bf16x8*)(K + (k0 + 16 + rr) * DH + 32 * e + 8 * G), q[e], s1);
        }
        float mx = fmaxf(fmaxf(fmaxf(s0[0], s0[1]), fmaxf(s0[2], s0[3])), fmaxf(fmaxf(s1[0], s1[1]), fmaxf(s1[2], s1[3])));
        mx = fmaxf(mx, __shfl_xor(mx, 16)); mx = fmaxf(mx, __shfl_xor(mx, 32));
        const float mn = fmaxf(m, mx), al = __builtin_amdgcn_exp2f(m - mn); m = mn;
        float ps = 0.f;
#pragma unroll
        for (int i = 0; i < 4; ++i) { s0[i] = __builtin_amdgcn_exp2f(s0[i] - mn); s1[i] = __builtin_amdgcn_exp2f(s1[i] - mn); ps += s0[i] + s1[i]; }
        ps += __shfl_xor(ps, 16); ps += __shfl_xor(ps, 32); l = l * al + ps;
        u32x4 pw = {cvtpk(s0[0], s0[1]), cvtpk(s0[2], s0[3]), cvtpk(s1[0], s1[1]), cvtpk(s1[2], s1[3])};
        const bf16x8 p = __builtin_bit_cast(bf16x8, pw);
#pragma unroll
        for (int c = 0; c < 8; ++c) {
          bf16x8 vf;
#pragma unroll
          for (int jj = 0; jj < 8; ++jj) vf[jj] = (short)V[(k0 + 16 * (jj >> 2) + 4 * G + (jj & 3)) * DH + 16 * c + rr];
          o[c] *= al;
          o[c] = MFMA16(vf, p, o[c]);
        }
      }
      const float il = 1.f / l;
      const unsigned rb = blk < 6u ? 6u * g + blk : 12u;
#pragma unroll
      for (int e = 0; e < 4; ++e) {
        u32x4 ow = {cvtpk(o[2 * e][0] * il, o[2 * e][1] * il), cvtpk(o[2 * e][2] * il, o[2 * e][3] * il), cvtpk(o[2 * e + 1][0] * il, o[2 * e + 1][1] * il), cvtpk(o[2 * e + 1][2] * il, o[2 * e + 1][3] * il)};
        *(GAS u32x4*)(Ob + ((h * 4u + e) * 104u + b * 13u + rb) * 1024u + lane * 16u) = ow;
      }
    }
    __threadfence();
    asm volatile("s_waitcnt vmcnt(0) lgkmcnt(0)" ::: "memory");
  }
  if (c.r >= 30u) {
    asm volatile("s_waitcnt vmcnt(0)\n\ts_barrier" ::: "memory");
    const unsigned lane = __builtin_amdgcn_mbcnt_hi(~0u, __builtin_amdgcn_mbcnt_lo(~0u, 0u));
    if (w == 0 && lane == 0) __hip_atomic_store(flags + (br * 8u + b) * 16u + j, tagu, __ATOMIC_RELAXED, __HIP_MEMORY_SCOPE_AGENT);
    {
      unsigned off = kh * 2621440u + WT_N * 262144u + ((((bid & 1u) * 2u + (c.r & 1u)) * 4u + w) * 64u + lane) * 128u;
#pragma unroll 1
      for (unsigned i = 0; i < (10u - WT_N) * 2u; ++i, off += 131072u) asm volatile("global_load_dword a255, %0, %1" :: "v"(off), "s"(wop) : "memory", "a255");
      asm volatile("s_waitcnt vmcnt(0)" ::: "memory");
    }
    if (w == 0) {
      if (bid == 255u) {
#pragma unroll 1
        for (unsigned spin = 0; spin < 0x400000u; ++spin) {
          bool ok = true;
#pragma unroll
          for (int i = 0; i < 4; ++i) ok &= __hip_atomic_load(flags + lane + 64 * i, __ATOMIC_RELAXED, __HIP_MEMORY_SCOPE_AGENT) == tagu;
          if (__all(ok)) break;
          __builtin_amdgcn_s_sleep(16);
        }
        if (lane == 0) __hip_atomic_store(flags + 256, tagu, __ATOMIC_RELAXED, __HIP_MEMORY_SCOPE_AGENT);
      }
    }
    return;
  }
  asm volatile(
".macro MM d,a,b,c\n"
"v_mfma_f32_16x16x32_bf16 \\d,\\a,\\b,\\c\n"
".endm\n"
".macro CV d,a,b\n"
"v_cvt_pk_bf16_f32 \\d,\\a,\\b\n"
".endm\n"
".macro TR d,a,o\n"
"ds_read_b64_tr_b16 \\d,\\a \\o\n"
".endm\n"
"v_mbcnt_lo_u32_b32 v8,-1,0\nv_mbcnt_hi_u32_b32 v8,-1,v8\nv_mov_b32 v12,0\nv_mov_b32 v13,0\nv_mov_b32 v14,0\nv_mov_b32 v15,0\nv_mov_b64 v[10:11],%9\n"
"s_mov_b64 s[44:45],%0\ns_and_b32 s45,s45,0xffff\ns_mov_b32 s46,%1\ns_mov_b32 s47,0x20000\ns_mov_b64 s[40:41],%2\ns_and_b32 s41,s41,0xffff\n"
"s_mov_b32 s42,%3\ns_mov_b32 s43,0x20000\ns_mov_b32 s48,0\ns_mov_b32 s49,4096\ns_mov_b32 s50,0\ns_mov_b32 s51,9216\ns_mov_b32 s52,18432\n"
"s_mov_b32 s53,27648\ns_lshr_b32 s58,%4,1\ns_mul_i32 s58,s58,40960\ns_and_b32 s59,%4,1\ns_mul_i32 s59,s59,5120\ns_add_u32 s54,s58,s59\n"
"s_lshl_b32 s55,%4,14\ns_add_u32 s55,s55,81920\ns_mov_b32 s56,7\nv_lshlrev_b32 v0,4,v8\nv_add_u32 v1,%5,v0\nv_add_u32 v2,%6,v0\nv_add_u32 v9,s58,v0\n"
"v_bfe_u32 v224,v8,2,3\nv_lshrrev_b32 v225,5,v8\nv_bfe_u32 v226,v8,4,1\nv_and_b32 v227,3,v8\nv_mul_u32_u24 v3,1280,v224\nv_lshl_add_u32 v3,v225,6,v3\n"
"v_lshlrev_b32 v4,1,v226\nv_xor_b32 v4,v4,v227\nv_lshl_add_u32 v3,v4,4,v3\ns_and_b32 s58,%10,0xffff\ns_lshr_b32 s59,%10,16\nv_and_b32 v231,7,v8\n"
"v_lshlrev_b32 v231,3,v231\nv_mov_b32 v230,s59\nv_mov_b32 v229,s58\nv_cmp_gt_u32 vcc,8,v8\ns_nop 1\nv_cndmask_b32 v230,v230,v229,vcc\n"
"v_add_u32 v231,v231,v230\ns_add_u32 m0,s55,0\ns_nop 0\nbuffer_load_dwordx4 v3,s[40:43],s50 offen offset:0 lds\n"
"buffer_load_dwordx4 v3,s[40:43],s51 offen offset:1024 lds\nbuffer_load_dwordx4 v3,s[40:43],s52 offen offset:2048 lds\n"
"buffer_load_dwordx4 v3,s[40:43],s53 offen offset:3072 lds\ns_add_u32 s50,s50,81920\ns_add_u32 s51,s51,81920\ns_add_u32 s52,s52,81920\n"
"s_add_u32 s53,s53,81920\ns_add_u32 m0,s55,4096\ns_nop 0\nbuffer_load_dwordx4 v3,s[40:43],s50 offen offset:0 lds\n"
"buffer_load_dwordx4 v3,s[40:43],s51 offen offset:1024 lds\nbuffer_load_dwordx4 v3,s[40:43],s52 offen offset:2048 lds\n"
"buffer_load_dwordx4 v3,s[40:43],s53 offen offset:3072 lds\ns_add_u32 s50,s50,81920\ns_add_u32 s51,s51,81920\ns_add_u32 s52,s52,81920\n"
"s_add_u32 s53,s53,81920\ns_add_u32 m0,s55,8192\ns_nop 0\nbuffer_load_dwordx4 v3,s[40:43],s50 offen offset:0 lds\n"
"buffer_load_dwordx4 v3,s[40:43],s51 offen offset:1024 lds\nbuffer_load_dwordx4 v3,s[40:43],s52 offen offset:2048 lds\n"
"buffer_load_dwordx4 v3,s[40:43],s53 offen offset:3072 lds\ns_add_u32 s50,s50,81920\ns_add_u32 s51,s51,81920\ns_add_u32 s52,s52,81920\n"
"s_add_u32 s53,s53,81920\nv_lshlrev_b32 v225,10,v225\nv_lshl_add_u32 v225,v224,6,v225\nv_lshl_add_u32 v225,v226,5,v225\nv_lshl_add_u32 v225,v227,3,v225\n"
"v_add_u32 v225,s55,v225\ns_and_b32 s58,%11,1\ns_lshl_b32 s58,s58,5\ns_and_b32 s59,%11,2\ns_lshl_b32 s59,s59,8\ns_or_b32 s58,s58,s59\n"
"v_xor_b32 v4,s58,v225\nv_xor_b32 v5,512,v4\nv_xor_b32 v6,32,v4\nv_xor_b32 v7,544,v4\nv_mfma_f32_32x32x16_bf16 v[16:31],v[12:15],v[12:15],0\n"
"v_mfma_f32_32x32x16_bf16 v[32:47],v[12:15],v[12:15],0\nv_mfma_f32_32x32x16_bf16 v[48:63],v[12:15],v[12:15],0\n"
"v_mfma_f32_32x32x16_bf16 v[64:79],v[12:15],v[12:15],0\nv_mfma_f32_32x32x16_bf16 v[80:95],v[12:15],v[12:15],0\n"
"v_mfma_f32_32x32x16_bf16 v[96:111],v[12:15],v[12:15],0\nv_mfma_f32_32x32x16_bf16 v[112:127],v[12:15],v[12:15],0\n"
"v_mfma_f32_32x32x16_bf16 v[128:143],v[12:15],v[12:15],0\nv_mfma_f32_32x32x16_bf16 v[144:159],v[12:15],v[12:15],0\ns_waitcnt vmcnt(12)\ns_barrier\n"
"s_cmp_lg_u32 %4,0\ns_cbranch_scc1 L_nopub_%=\ns_mov_b64 exec,1\nglobal_store_dwordx2 v12,v[10:11],%7 sc1\ns_mov_b64 exec,-1\nL_nopub_%=:\n"
"s_mov_b32 s61,771\ns_getpc_b64 s[62:63]\ns_add_u32 s62,s62,L_first_%=-.\ns_addc_u32 s63,s63,0\nL_wait_%=:\ns_mov_b32 s57,0x400000\nL_waitl_%=:\n"
"global_load_dwordx2 v[234:235],v231,%8 sc1\ns_waitcnt vmcnt(0)\nv_cmp_ne_u64 vcc,v[234:235],v[10:11]\ns_nop 4\ns_and_b32 s60,vcc_lo,0xffff\n"
"s_and_b32 s58,s60,s61\ns_cbranch_scc0 L_waitx_%=\ns_sleep 1\ns_sub_u32 s57,s57,1\ns_cmp_lg_u32 s57,0\ns_cbranch_scc1 L_waitl_%=\nL_waitx_%=:\n"
"s_setpc_b64 s[62:63]\nL_first_%=:\ns_getpc_b64 s[62:63]\ns_add_u32 s62,s62,L_back_%=-.\ns_addc_u32 s63,s63,0\ns_waitcnt vmcnt(0)\ns_add_u32 m0,s54,0\n"
"s_nop 0\nbuffer_load_dwordx4 v0,s[44:47],s48 offen offset:0 sc1 lds\nbuffer_load_dwordx4 v0,s[44:47],s48 offen offset:1024 sc1 lds\n"
"buffer_load_dwordx4 v0,s[44:47],s48 offen offset:2048 sc1 lds\nbuffer_load_dwordx4 v1,s[44:47],s48 offen offset:3072 sc1 lds\ns_add_u32 m0,s54,4096\n"
"s_nop 0\nbuffer_load_dwordx4 v2,s[44:47],s49 offen offset:0 sc1 lds\ns_add_u32 s48,s48,212992\ns_add_u32 s49,s49,212992\ns_add_u32 m0,s54,10240\n"
"s_nop 0\nbuffer_load_dwordx4 v0,s[44:47],s48 offen offset:0 sc1 lds\nbuffer_load_dwordx4 v0,s[44:47],s48 offen offset:1024 sc1 lds\n"
"buffer_load_dwordx4 v0,s[44:47],s48 offen offset:2048 sc1 lds\nbuffer_load_dwordx4 v1,s[44:47],s48 offen offset:3072 sc1 lds\ns_add_u32 m0,s54,14336\n"
"s_nop 0\nbuffer_load_dwordx4 v2,s[44:47],s49 offen offset:0 sc1 lds\ns_add_u32 s48,s48,212992\ns_add_u32 s49,s49,212992\ns_add_u32 m0,s54,20480\n"
"s_nop 0\nbuffer_load_dwordx4 v0,s[44:47],s48 offen offset:0 sc1 lds\nbuffer_load_dwordx4 v0,s[44:47],s48 offen offset:1024 sc1 lds\n"
"buffer_load_dwordx4 v0,s[44:47],s48 offen offset:2048 sc1 lds\nbuffer_load_dwordx4 v1,s[44:47],s48 offen offset:3072 sc1 lds\ns_add_u32 m0,s54,24576\n"
"s_nop 0\nbuffer_load_dwordx4 v2,s[44:47],s49 offen offset:0 sc1 lds\ns_add_u32 s48,s48,212992\ns_add_u32 s49,s49,212992\ns_add_u32 m0,s55,12288\n"
"s_nop 0\nbuffer_load_dwordx4 v3,s[40:43],s50 offen offset:0 lds\nbuffer_load_dwordx4 v3,s[40:43],s51 offen offset:1024 lds\n"
"buffer_load_dwordx4 v3,s[40:43],s52 offen offset:2048 lds\nbuffer_load_dwordx4 v3,s[40:43],s53 offen offset:3072 lds\ns_add_u32 s50,s50,81920\n"
"s_add_u32 s51,s51,81920\ns_add_u32 s52,s52,81920\ns_add_u32 s53,s53,81920\ns_add_u32 m0,s54,30720\ns_nop 0\n"
"buffer_load_dwordx4 v0,s[44:47],s48 offen offset:0 sc1 lds\nbuffer_load_dwordx4 v0,s[44:47],s48 offen offset:1024 sc1 lds\n"
"buffer_load_dwordx4 v0,s[44:47],s48 offen offset:2048 sc1 lds\nbuffer_load_dwordx4 v1,s[44:47],s48 offen offset:3072 sc1 lds\ns_add_u32 m0,s54,34816\n"
"s_nop 0\nbuffer_load_dwordx4 v2,s[44:47],s49 offen offset:0 sc1 lds\ns_add_u32 s48,s48,212992\ns_add_u32 s49,s49,212992\nTR v[160:161],v4 offset:0\n"
"TR v[162:163],v4 offset:2048\nTR v[164:165],v5 offset:0\nTR v[166:167],v5 offset:2048\nTR v[168:169],v6 offset:0\nTR v[170:171],v6 offset:2048\n"
"TR v[172:173],v7 offset:0\nTR v[174:175],v7 offset:2048\nTR v[176:177],v4 offset:4096\nTR v[178:179],v4 offset:6144\nTR v[180:181],v5 offset:4096\n"
"TR v[182:183],v5 offset:6144\nTR v[184:185],v6 offset:4096\nTR v[186:187],v6 offset:6144\nTR v[188:189],v7 offset:4096\nTR v[190:191],v7 offset:6144\n"
"s_waitcnt vmcnt(14)\ns_barrier\nds_read_b128 a[0:3],v9 offset:0\nds_read_b128 a[4:7],v9 offset:1024\nds_read_b128 a[8:11],v9 offset:2048\n"
"ds_read_b128 a[12:15],v9 offset:3072\nds_read_b128 a[16:19],v9 offset:4096\nds_read_b128 a[20:23],v9 offset:5120\nds_read_b128 a[24:27],v9 offset:6144\n"
"ds_read_b128 a[28:31],v9 offset:7168\nds_read_b128 a[32:35],v9 offset:8192\nds_read_b128 a[36:39],v9 offset:10240\n"
"ds_read_b128 a[40:43],v9 offset:11264\nds_read_b128 a[44:47],v9 offset:12288\nds_read_b128 a[48:51],v9 offset:13312\n"
"ds_read_b128 a[52:55],v9 offset:14336\nds_read_b128 a[56:59],v9 offset:15360\nds_read_b128 a[60:63],v9 offset:16384\n"
"ds_read_b128 a[64:67],v9 offset:17408\nds_read_b128 a[68:71],v9 offset:18432\ns_waitcnt lgkmcnt(9)\ns_barrier\ns_add_u32 m0,s55,0\ns_nop 0\n"
"buffer_load_dwordx4 v3,s[40:43],s50 offen offset:0 lds\nbuffer_load_dwordx4 v3,s[40:43],s51 offen offset:1024 lds\n"
"buffer_load_dwordx4 v3,s[40:43],s52 offen offset:2048 lds\nbuffer_load_dwordx4 v3,s[40:43],s53 offen offset:3072 lds\ns_add_u32 s50,s50,81920\n"
"s_add_u32 s51,s51,81920\ns_add_u32 s52,s52,81920\ns_add_u32 s53,s53,81920\ns_add_u32 m0,s54,0\ns_nop 0\n"
"buffer_load_dwordx4 v0,s[44:47],s48 offen offset:0 sc1 lds\nbuffer_load_dwordx4 v0,s[44:47],s48 offen offset:1024 sc1 lds\n"
"buffer_load_dwordx4 v0,s[44:47],s48 offen offset:2048 sc1 lds\nbuffer_load_dwordx4 v1,s[44:47],s48 offen offset:3072 sc1 lds\ns_add_u32 m0,s54,4096\n"
"s_nop 0\nbuffer_load_dwordx4 v2,s[44:47],s49 offen offset:0 sc1 lds\ns_add_u32 s48,s48,212992\ns_add_u32 s49,s49,212992\n.p2align 6\nL_k2_loop_%=:\n"
"s_waitcnt vmcnt(18) lgkmcnt(0)\ns_barrier\nMM v[16:19],v[160:163],a[0:3],v[16:19]\nTR v[192:193],v4 offset:8192\ns_add_u32 m0,s55,4096\n"
"MM v[16:19],v[176:179],a[36:39],v[16:19]\nTR v[194:195],v4 offset:10240\nMM v[20:23],v[180:183],a[36:39],v[20:23]\n"
"buffer_load_dwordx4 v3,s[40:43],s50 offen offset:0 lds\nMM v[20:23],v[164:167],a[0:3],v[20:23]\nTR v[196:197],v5 offset:8192\n"
"MM v[24:27],v[168:171],a[0:3],v[24:27]\nbuffer_load_dwordx4 v3,s[40:43],s51 offen offset:1024 lds\nMM v[24:27],v[184:187],a[36:39],v[24:27]\n"
"TR v[198:199],v5 offset:10240\nMM v[28:31],v[188:191],a[36:39],v[28:31]\nbuffer_load_dwordx4 v3,s[40:43],s52 offen offset:2048 lds\n"
"MM v[28:31],v[172:175],a[0:3],v[28:31]\nTR v[200:201],v6 offset:8192\nMM v[32:35],v[160:163],a[4:7],v[32:35]\nTR v[202:203],v6 offset:10240\n"
"MM v[32:35],v[176:179],a[40:43],v[32:35]\nbuffer_load_dwordx4 v3,s[40:43],s53 offen offset:3072 lds\nMM v[36:39],v[180:183],a[40:43],v[36:39]\n"
"TR v[204:205],v7 offset:8192\nMM v[36:39],v[164:167],a[4:7],v[36:39]\ns_add_u32 s50,s50,81920\ns_add_u32 s51,s51,81920\n"
"MM v[40:43],v[168:171],a[4:7],v[40:43]\nTR v[206:207],v7 offset:10240\nMM v[40:43],v[184:187],a[40:43],v[40:43]\ns_add_u32 s52,s52,81920\n"
"s_add_u32 s53,s53,81920\nMM v[44:47],v[188:191],a[40:43],v[44:47]\nds_read_b128 a[72:75],v9 offset:20480\nMM v[44:47],v[172:175],a[4:7],v[44:47]\n"
"ds_read_b128 a[76:79],v9 offset:21504\ns_add_u32 m0,s54,10240\nMM v[48:51],v[160:163],a[8:11],v[48:51]\nMM v[48:51],v[176:179],a[44:47],v[48:51]\n"
"ds_read_b128 a[80:83],v9 offset:22528\nMM v[52:55],v[180:183],a[44:47],v[52:55]\nbuffer_load_dwordx4 v0,s[44:47],s48 offen offset:0 sc1 lds\n"
"MM v[52:55],v[164:167],a[8:11],v[52:55]\nds_read_b128 a[84:87],v9 offset:23552\nMM v[56:59],v[168:171],a[8:11],v[56:59]\n"
"buffer_load_dwordx4 v0,s[44:47],s48 offen offset:1024 sc1 lds\nMM v[56:59],v[184:187],a[44:47],v[56:59]\nds_read_b128 a[88:91],v9 offset:24576\n"
"MM v[60:63],v[188:191],a[44:47],v[60:63]\nds_read_b128 a[92:95],v9 offset:25600\nMM v[60:63],v[172:175],a[8:11],v[60:63]\n"
"buffer_load_dwordx4 v0,s[44:47],s48 offen offset:2048 sc1 lds\nMM v[64:67],v[160:163],a[12:15],v[64:67]\nds_read_b128 a[96:99],v9 offset:26624\n"
"MM v[64:67],v[176:179],a[48:51],v[64:67]\nbuffer_load_dwordx4 v1,s[44:47],s48 offen offset:3072 sc1 lds\nMM v[68:71],v[180:183],a[48:51],v[68:71]\n"
"ds_read_b128 a[100:103],v9 offset:27648\nMM v[68:71],v[164:167],a[12:15],v[68:71]\ns_add_u32 m0,s54,14336\nMM v[72:75],v[168:171],a[12:15],v[72:75]\n"
"ds_read_b128 a[104:107],v9 offset:28672\nMM v[72:75],v[184:187],a[48:51],v[72:75]\nbuffer_load_dwordx4 v2,s[44:47],s49 offen offset:0 sc1 lds\n"
"MM v[76:79],v[188:191],a[48:51],v[76:79]\nMM v[76:79],v[172:175],a[12:15],v[76:79]\ns_add_u32 s48,s48,212992\ns_add_u32 s49,s49,212992\n"
"MM v[80:83],v[160:163],a[16:19],v[80:83]\nMM v[80:83],v[176:179],a[52:55],v[80:83]\nMM v[84:87],v[180:183],a[52:55],v[84:87]\n"
"MM v[84:87],v[164:167],a[16:19],v[84:87]\ns_waitcnt vmcnt(18) lgkmcnt(0)\ns_barrier\nMM v[88:91],v[168:171],a[16:19],v[88:91]\n"
"TR v[208:209],v4 offset:12288\ns_add_u32 m0,s55,8192\nMM v[88:91],v[184:187],a[52:55],v[88:91]\nTR v[210:211],v4 offset:14336\n"
"MM v[92:95],v[188:191],a[52:55],v[92:95]\nbuffer_load_dwordx4 v3,s[40:43],s50 offen offset:0 lds\nMM v[92:95],v[172:175],a[16:19],v[92:95]\n"
"TR v[212:213],v5 offset:12288\nMM v[96:99],v[160:163],a[20:23],v[96:99]\nbuffer_load_dwordx4 v3,s[40:43],s51 offen offset:1024 lds\n"
"MM v[96:99],v[176:179],a[56:59],v[96:99]\nTR v[214:215],v5 offset:14336\nMM v[100:103],v[180:183],a[56:59],v[100:103]\n"
"buffer_load_dwordx4 v3,s[40:43],s52 offen offset:2048 lds\nMM v[100:103],v[164:167],a[20:23],v[100:103]\nTR v[216:217],v6 offset:12288\n"
"MM v[104:107],v[168:171],a[20:23],v[104:107]\nTR v[218:219],v6 offset:14336\nMM v[104:107],v[184:187],a[56:59],v[104:107]\n"
"buffer_load_dwordx4 v3,s[40:43],s53 offen offset:3072 lds\nMM v[108:111],v[188:191],a[56:59],v[108:111]\nTR v[220:221],v7 offset:12288\n"
"MM v[108:111],v[172:175],a[20:23],v[108:111]\ns_add_u32 s50,s50,81920\ns_add_u32 s51,s51,81920\nMM v[112:115],v[160:163],a[24:27],v[112:115]\n"
"TR v[222:223],v7 offset:14336\nMM v[112:115],v[176:179],a[60:63],v[112:115]\ns_add_u32 s52,s52,81920\ns_add_u32 s53,s53,81920\n"
"MM v[116:119],v[180:183],a[60:63],v[116:119]\nds_read_b128 a[108:111],v9 offset:30720\nMM v[116:119],v[164:167],a[24:27],v[116:119]\n"
"ds_read_b128 a[112:115],v9 offset:31744\ns_add_u32 m0,s54,20480\nMM v[120:123],v[168:171],a[24:27],v[120:123]\n"
"MM v[120:123],v[184:187],a[60:63],v[120:123]\nds_read_b128 a[116:119],v9 offset:32768\nMM v[124:127],v[188:191],a[60:63],v[124:127]\n"
"buffer_load_dwordx4 v0,s[44:47],s48 offen offset:0 sc1 lds\nMM v[124:127],v[172:175],a[24:27],v[124:127]\nds_read_b128 a[120:123],v9 offset:33792\n"
"MM v[128:131],v[160:163],a[28:31],v[128:131]\nbuffer_load_dwordx4 v0,s[44:47],s48 offen offset:1024 sc1 lds\n"
"MM v[128:131],v[176:179],a[64:67],v[128:131]\nds_read_b128 a[124:127],v9 offset:34816\nMM v[132:135],v[180:183],a[64:67],v[132:135]\n"
"ds_read_b128 a[128:131],v9 offset:35840\nMM v[132:135],v[164:167],a[28:31],v[132:135]\nbuffer_load_dwordx4 v0,s[44:47],s48 offen offset:2048 sc1 lds\n"
"MM v[136:139],v[168:171],a[28:31],v[136:139]\nds_read_b128 a[132:135],v9 offset:36864\nMM v[136:139],v[184:187],a[64:67],v[136:139]\n"
"buffer_load_dwordx4 v1,s[44:47],s48 offen offset:3072 sc1 lds\nMM v[140:143],v[188:191],a[64:67],v[140:143]\nds_read_b128 a[136:139],v9 offset:37888\n"
"MM v[140:143],v[172:175],a[28:31],v[140:143]\ns_add_u32 m0,s54,24576\nMM v[144:147],v[160:163],a[32:35],v[144:147]\n"
"ds_read_b128 a[140:143],v9 offset:38912\nMM v[144:147],v[176:179],a[68:71],v[144:147]\nbuffer_load_dwordx4 v2,s[44:47],s49 offen offset:0 sc1 lds\n"
"MM v[148:151],v[180:183],a[68:71],v[148:151]\nMM v[148:151],v[164:167],a[32:35],v[148:151]\ns_add_u32 s48,s48,212992\ns_add_u32 s49,s49,212992\n"
"MM v[152:155],v[168:171],a[32:35],v[152:155]\nMM v[152:155],v[184:187],a[68:71],v[152:155]\nMM v[156:159],v[188:191],a[68:71],v[156:159]\n"
"MM v[156:159],v[172:175],a[32:35],v[156:159]\ns_waitcnt vmcnt(18) lgkmcnt(0)\ns_barrier\nMM v[16:19],v[192:195],a[72:75],v[16:19]\n"
"TR v[160:161],v4 offset:0\ns_add_u32 m0,s55,12288\nMM v[16:19],v[208:211],a[108:111],v[16:19]\nTR v[162:163],v4 offset:2048\n"
"MM v[20:23],v[212:215],a[108:111],v[20:23]\nbuffer_load_dwordx4 v3,s[40:43],s50 offen offset:0 lds\nMM v[20:23],v[196:199],a[72:75],v[20:23]\n"
"TR v[164:165],v5 offset:0\nMM v[24:27],v[200:203],a[72:75],v[24:27]\nbuffer_load_dwordx4 v3,s[40:43],s51 offen offset:1024 lds\n"
"MM v[24:27],v[216:219],a[108:111],v[24:27]\nTR v[166:167],v5 offset:2048\nMM v[28:31],v[220:223],a[108:111],v[28:31]\n"
"buffer_load_dwordx4 v3,s[40:43],s52 offen offset:2048 lds\nMM v[28:31],v[204:207],a[72:75],v[28:31]\nTR v[168:169],v6 offset:0\n"
"MM v[32:35],v[192:195],a[76:79],v[32:35]\nTR v[170:171],v6 offset:2048\nMM v[32:35],v[208:211],a[112:115],v[32:35]\n"
"buffer_load_dwordx4 v3,s[40:43],s53 offen offset:3072 lds\nMM v[36:39],v[212:215],a[112:115],v[36:39]\nTR v[172:173],v7 offset:0\n"
"MM v[36:39],v[196:199],a[76:79],v[36:39]\ns_add_u32 s50,s50,81920\ns_add_u32 s51,s51,81920\nMM v[40:43],v[200:203],a[76:79],v[40:43]\n"
"TR v[174:175],v7 offset:2048\nMM v[40:43],v[216:219],a[112:115],v[40:43]\ns_add_u32 s52,s52,81920\ns_add_u32 s53,s53,81920\n"
"MM v[44:47],v[220:223],a[112:115],v[44:47]\nds_read_b128 a[0:3],v9 offset:0\nMM v[44:47],v[204:207],a[76:79],v[44:47]\n"
"ds_read_b128 a[4:7],v9 offset:1024\ns_add_u32 m0,s54,30720\nMM v[48:51],v[192:195],a[80:83],v[48:51]\nMM v[48:51],v[208:211],a[116:119],v[48:51]\n"
"ds_read_b128 a[8:11],v9 offset:2048\nMM v[52:55],v[212:215],a[116:119],v[52:55]\nbuffer_load_dwordx4 v0,s[44:47],s48 offen offset:0 sc1 lds\n"
"MM v[52:55],v[196:199],a[80:83],v[52:55]\nds_read_b128 a[12:15],v9 offset:3072\nMM v[56:59],v[200:203],a[80:83],v[56:59]\n"
"buffer_load_dwordx4 v0,s[44:47],s48 offen offset:1024 sc1 lds\nMM v[56:59],v[216:219],a[116:119],v[56:59]\nds_read_b128 a[16:19],v9 offset:4096\n"
"MM v[60:63],v[220:223],a[116:119],v[60:63]\nds_read_b128 a[20:23],v9 offset:5120\nMM v[60:63],v[204:207],a[80:83],v[60:63]\n"
"buffer_load_dwordx4 v0,s[44:47],s48 offen offset:2048 sc1 lds\nMM v[64:67],v[192:195],a[84:87],v[64:67]\nds_read_b128 a[24:27],v9 offset:6144\n"
"MM v[64:67],v[208:211],a[120:123],v[64:67]\nbuffer_load_dwordx4 v1,s[44:47],s48 offen offset:3072 sc1 lds\nMM v[68:71],v[212:215],a[120:123],v[68:71]\n"
"ds_read_b128 a[28:31],v9 offset:7168\nMM v[68:71],v[196:199],a[84:87],v[68:71]\ns_add_u32 m0,s54,34816\nMM v[72:75],v[200:203],a[84:87],v[72:75]\n"
"ds_read_b128 a[32:35],v9 offset:8192\nMM v[72:75],v[216:219],a[120:123],v[72:75]\nbuffer_load_dwordx4 v2,s[44:47],s49 offen offset:0 sc1 lds\n"
"MM v[76:79],v[220:223],a[120:123],v[76:79]\nMM v[76:79],v[204:207],a[84:87],v[76:79]\ns_add_u32 s48,s48,212992\ns_add_u32 s49,s49,212992\n"
"MM v[80:83],v[192:195],a[88:91],v[80:83]\nMM v[80:83],v[208:211],a[124:127],v[80:83]\nMM v[84:87],v[212:215],a[124:127],v[84:87]\n"
"MM v[84:87],v[196:199],a[88:91],v[84:87]\ns_lshl_b32 s61,s61,1\ns_and_b32 s61,s61,0xfefe\ns_and_b32 s58,s60,s61\ns_cbranch_scc1 L_wait_%=\nL_back_%=:\n"
"s_waitcnt vmcnt(18) lgkmcnt(0)\ns_barrier\nMM v[88:91],v[200:203],a[88:91],v[88:91]\nTR v[176:177],v4 offset:4096\ns_add_u32 m0,s55,0\n"
"MM v[88:91],v[216:219],a[124:127],v[88:91]\nTR v[178:179],v4 offset:6144\nMM v[92:95],v[220:223],a[124:127],v[92:95]\n"
"buffer_load_dwordx4 v3,s[40:43],s50 offen offset:0 lds\nMM v[92:95],v[204:207],a[88:91],v[92:95]\nTR v[180:181],v5 offset:4096\n"
"MM v[96:99],v[192:195],a[92:95],v[96:99]\nbuffer_load_dwordx4 v3,s[40:43],s51 offen offset:1024 lds\nMM v[96:99],v[208:211],a[128:131],v[96:99]\n"
"TR v[182:183],v5 offset:6144\nMM v[100:103],v[212:215],a[128:131],v[100:103]\nbuffer_load_dwordx4 v3,s[40:43],s52 offen offset:2048 lds\n"
"MM v[100:103],v[196:199],a[92:95],v[100:103]\nTR v[184:185],v6 offset:4096\nMM v[104:107],v[200:203],a[92:95],v[104:107]\nTR v[186:187],v6 offset:6144\n"
"MM v[104:107],v[216:219],a[128:131],v[104:107]\nbuffer_load_dwordx4 v3,s[40:43],s53 offen offset:3072 lds\n"
"MM v[108:111],v[220:223],a[128:131],v[108:111]\nTR v[188:189],v7 offset:4096\nMM v[108:111],v[204:207],a[92:95],v[108:111]\ns_add_u32 s50,s50,81920\n"
"s_add_u32 s51,s51,81920\nMM v[112:115],v[192:195],a[96:99],v[112:115]\nTR v[190:191],v7 offset:6144\nMM v[112:115],v[208:211],a[132:135],v[112:115]\n"
"s_add_u32 s52,s52,81920\ns_add_u32 s53,s53,81920\nMM v[116:119],v[212:215],a[132:135],v[116:119]\nds_read_b128 a[36:39],v9 offset:10240\n"
"MM v[116:119],v[196:199],a[96:99],v[116:119]\nds_read_b128 a[40:43],v9 offset:11264\ns_add_u32 m0,s54,0\nMM v[120:123],v[200:203],a[96:99],v[120:123]\n"
"MM v[120:123],v[216:219],a[132:135],v[120:123]\nds_read_b128 a[44:47],v9 offset:12288\nMM v[124:127],v[220:223],a[132:135],v[124:127]\n"
"buffer_load_dwordx4 v0,s[44:47],s48 offen offset:0 sc1 lds\nMM v[124:127],v[204:207],a[96:99],v[124:127]\nds_read_b128 a[48:51],v9 offset:13312\n"
"MM v[128:131],v[192:195],a[100:103],v[128:131]\nbuffer_load_dwordx4 v0,s[44:47],s48 offen offset:1024 sc1 lds\n"
"MM v[128:131],v[208:211],a[136:139],v[128:131]\nds_read_b128 a[52:55],v9 offset:14336\nMM v[132:135],v[212:215],a[136:139],v[132:135]\n"
"ds_read_b128 a[56:59],v9 offset:15360\nMM v[132:135],v[196:199],a[100:103],v[132:135]\nbuffer_load_dwordx4 v0,s[44:47],s48 offen offset:2048 sc1 lds\n"
"MM v[136:139],v[200:203],a[100:103],v[136:139]\nds_read_b128 a[60:63],v9 offset:16384\nMM v[136:139],v[216:219],a[136:139],v[136:139]\n"
"buffer_load_dwordx4 v1,s[44:47],s48 offen offset:3072 sc1 lds\nMM v[140:143],v[220:223],a[136:139],v[140:143]\nds_read_b128 a[64:67],v9 offset:17408\n"
"MM v[140:143],v[204:207],a[100:103],v[140:143]\ns_add_u32 m0,s54,4096\nMM v[144:147],v[192:195],a[104:107],v[144:147]\n"
"ds_read_b128 a[68:71],v9 offset:18432\nMM v[144:147],v[208:211],a[140:143],v[144:147]\nbuffer_load_dwordx4 v2,s[44:47],s49 offen offset:0 sc1 lds\n"
"MM v[148:151],v[212:215],a[140:143],v[148:151]\nMM v[148:151],v[196:199],a[104:107],v[148:151]\ns_add_u32 s48,s48,212992\ns_add_u32 s49,s49,212992\n"
"MM v[152:155],v[200:203],a[104:107],v[152:155]\nMM v[152:155],v[216:219],a[140:143],v[152:155]\nMM v[156:159],v[220:223],a[140:143],v[156:159]\n"
"MM v[156:159],v[204:207],a[104:107],v[156:159]\ns_sub_u32 s56,s56,1\ns_cmp_lg_u32 s56,0\ns_cbranch_scc1 L_k2_loop_%=\ns_waitcnt vmcnt(0) lgkmcnt(0)\n"
"s_barrier\nMM v[28:31],v[172:175],a[0:3],v[28:31]\nTR v[192:193],v4 offset:8192\nTR v[194:195],v4 offset:10240\n"
"MM v[28:31],v[188:191],a[36:39],v[28:31]\nTR v[196:197],v5 offset:8192\nTR v[198:199],v5 offset:10240\nMM v[44:47],v[188:191],a[40:43],v[44:47]\n"
"TR v[200:201],v6 offset:8192\nTR v[202:203],v6 offset:10240\nMM v[44:47],v[172:175],a[4:7],v[44:47]\nTR v[204:205],v7 offset:8192\n"
"TR v[206:207],v7 offset:10240\nMM v[60:63],v[172:175],a[8:11],v[60:63]\nds_read_b128 a[72:75],v9 offset:20480\nds_read_b128 a[76:79],v9 offset:21504\n"
"MM v[60:63],v[188:191],a[44:47],v[60:63]\nds_read_b128 a[80:83],v9 offset:22528\nds_read_b128 a[84:87],v9 offset:23552\n"
"MM v[76:79],v[188:191],a[48:51],v[76:79]\nds_read_b128 a[88:91],v9 offset:24576\nds_read_b128 a[92:95],v9 offset:25600\n"
"MM v[76:79],v[172:175],a[12:15],v[76:79]\nds_read_b128 a[96:99],v9 offset:26624\nds_read_b128 a[100:103],v9 offset:27648\n"
"MM v[92:95],v[172:175],a[16:19],v[92:95]\nds_read_b128 a[104:107],v9 offset:28672\nTR v[208:209],v4 offset:12288\n"
"MM v[92:95],v[188:191],a[52:55],v[92:95]\nTR v[210:211],v4 offset:14336\nTR v[212:213],v5 offset:12288\nMM v[108:111],v[188:191],a[56:59],v[108:111]\n"
"TR v[214:215],v5 offset:14336\nTR v[216:217],v6 offset:12288\nMM v[108:111],v[172:175],a[20:23],v[108:111]\nTR v[218:219],v6 offset:14336\n"
"TR v[220:221],v7 offset:12288\nMM v[124:127],v[172:175],a[24:27],v[124:127]\nTR v[222:223],v7 offset:14336\nds_read_b128 a[108:111],v9 offset:30720\n"
"MM v[124:127],v[188:191],a[60:63],v[124:127]\nds_read_b128 a[112:115],v9 offset:31744\nds_read_b128 a[116:119],v9 offset:32768\n"
"MM v[140:143],v[188:191],a[64:67],v[140:143]\nds_read_b128 a[120:123],v9 offset:33792\nds_read_b128 a[124:127],v9 offset:34816\n"
"MM v[140:143],v[172:175],a[28:31],v[140:143]\nds_read_b128 a[128:131],v9 offset:35840\nds_read_b128 a[132:135],v9 offset:36864\n"
"MM v[156:159],v[172:175],a[32:35],v[156:159]\nds_read_b128 a[136:139],v9 offset:37888\nds_read_b128 a[140:143],v9 offset:38912\n"
"MM v[156:159],v[188:191],a[68:71],v[156:159]\nMM v[20:23],v[164:167],a[0:3],v[20:23]\nMM v[20:23],v[180:183],a[36:39],v[20:23]\n"
"MM v[36:39],v[180:183],a[40:43],v[36:39]\ns_mul_i32 s58,%4,18432\nv_add_u32 v1,s58,v0\nMM v[36:39],v[164:167],a[4:7],v[36:39]\ns_xor_b32 s58,%4,2\n"
"s_mul_i32 s58,s58,18432\nMM v[52:55],v[164:167],a[8:11],v[52:55]\nv_add_u32 v2,s58,v0\nMM v[52:55],v[180:183],a[44:47],v[52:55]\n"
"MM v[68:71],v[180:183],a[48:51],v[68:71]\nMM v[68:71],v[164:167],a[12:15],v[68:71]\nMM v[84:87],v[164:167],a[16:19],v[84:87]\n"
"MM v[84:87],v[180:183],a[52:55],v[84:87]\nMM v[100:103],v[180:183],a[56:59],v[100:103]\nMM v[100:103],v[164:167],a[20:23],v[100:103]\n"
"MM v[116:119],v[164:167],a[24:27],v[116:119]\nMM v[116:119],v[180:183],a[60:63],v[116:119]\nMM v[132:135],v[180:183],a[64:67],v[132:135]\n"
"MM v[132:135],v[164:167],a[28:31],v[132:135]\nMM v[148:151],v[164:167],a[32:35],v[148:151]\nMM v[148:151],v[180:183],a[68:71],v[148:151]\n"
"s_waitcnt lgkmcnt(0)\ns_barrier\nMM v[28:31],v[204:207],a[72:75],v[28:31]\nv_add_u32 v3,4096,v0\nv_add_u32 v4,8192,v0\n"
"MM v[28:31],v[220:223],a[108:111],v[28:31]\ns_mov_b64 s[44:45],%16\ns_and_b32 s45,s45,0xffff\nMM v[44:47],v[220:223],a[112:115],v[44:47]\n"
"s_mov_b32 s46,%17\nv_and_b32 v7,15,v8\nMM v[44:47],v[204:207],a[76:79],v[44:47]\nv_mul_u32_u24 v7,2560,v7\nv_lshrrev_b32 v9,4,v8\n"
"MM v[60:63],v[204:207],a[80:83],v[60:63]\nv_lshl_add_u32 v7,v9,3,v7\ns_lshl_b32 s58,%11,5\nMM v[60:63],v[220:223],a[116:119],v[60:63]\n"
"MM v[76:79],v[220:223],a[120:123],v[76:79]\nMM v[76:79],v[204:207],a[84:87],v[76:79]\nMM v[92:95],v[204:207],a[88:91],v[92:95]\n"
"MM v[92:95],v[220:223],a[124:127],v[92:95]\nMM v[108:111],v[220:223],a[128:131],v[108:111]\nds_write_b128 v2,v[28:31] offset:0\n"
"MM v[108:111],v[204:207],a[92:95],v[108:111]\nMM v[124:127],v[204:207],a[96:99],v[124:127]\nds_write_b128 v2,v[44:47] offset:1024\n"
"MM v[124:127],v[220:223],a[132:135],v[124:127]\nMM v[140:143],v[220:223],a[136:139],v[140:143]\nds_write_b128 v2,v[60:63] offset:2048\n"
"MM v[140:143],v[204:207],a[100:103],v[140:143]\nMM v[156:159],v[204:207],a[104:107],v[156:159]\nds_write_b128 v2,v[76:79] offset:3072\n"
"MM v[156:159],v[220:223],a[140:143],v[156:159]\nMM v[20:23],v[196:199],a[72:75],v[20:23]\nds_write_b128 v2,v[92:95] offset:4096\n"
"MM v[20:23],v[212:215],a[108:111],v[20:23]\nMM v[36:39],v[212:215],a[112:115],v[36:39]\nds_write_b128 v2,v[108:111] offset:5120\n"
"MM v[36:39],v[196:199],a[76:79],v[36:39]\nMM v[52:55],v[196:199],a[80:83],v[52:55]\nds_write_b128 v2,v[124:127] offset:6144\n"
"MM v[52:55],v[212:215],a[116:119],v[52:55]\nMM v[68:71],v[212:215],a[120:123],v[68:71]\nds_write_b128 v2,v[140:143] offset:7168\n"
"MM v[68:71],v[196:199],a[84:87],v[68:71]\nMM v[84:87],v[196:199],a[88:91],v[84:87]\nds_write_b128 v2,v[156:159] offset:8192\n"
"MM v[84:87],v[212:215],a[124:127],v[84:87]\nMM v[100:103],v[212:215],a[128:131],v[100:103]\nMM v[100:103],v[196:199],a[92:95],v[100:103]\n"
"MM v[116:119],v[196:199],a[96:99],v[116:119]\nMM v[116:119],v[212:215],a[132:135],v[116:119]\nMM v[132:135],v[212:215],a[136:139],v[132:135]\n"
"MM v[132:135],v[196:199],a[100:103],v[132:135]\nMM v[148:151],v[196:199],a[104:107],v[148:151]\ns_waitcnt lgkmcnt(0)\ns_barrier\n"
"MM v[148:151],v[212:215],a[140:143],v[148:151]\nds_read_b128 v[28:31],v1 offset:0\nds_read_b128 v[44:47],v1 offset:1024\n"
"MM v[24:27],v[168:171],a[0:3],v[24:27]\nds_read_b128 v[60:63],v1 offset:2048\nds_read_b128 v[76:79],v1 offset:3072\n"
"MM v[24:27],v[184:187],a[36:39],v[24:27]\nds_read_b128 v[92:95],v1 offset:4096\nds_read_b128 v[108:111],v1 offset:5120\n"
"MM v[24:27],v[200:203],a[72:75],v[24:27]\nds_read_b128 v[124:127],v1 offset:6144\nds_read_b128 v[140:143],v1 offset:7168\n"
"MM v[24:27],v[216:219],a[108:111],v[24:27]\nds_read_b128 v[156:159],v1 offset:8192\nMM v[40:43],v[216:219],a[112:115],v[40:43]\n"
"MM v[40:43],v[200:203],a[76:79],v[40:43]\nMM v[40:43],v[184:187],a[40:43],v[40:43]\nMM v[40:43],v[168:171],a[4:7],v[40:43]\n"
"MM v[56:59],v[168:171],a[8:11],v[56:59]\nMM v[56:59],v[184:187],a[44:47],v[56:59]\ns_waitcnt lgkmcnt(0)\nv_add_f32 v20,v20,v28\nv_add_f32 v21,v21,v29\n"
"MM v[56:59],v[200:203],a[80:83],v[56:59]\nv_add_f32 v22,v22,v30\nv_add_f32 v23,v23,v31\nMM v[56:59],v[216:219],a[116:119],v[56:59]\n"
"global_store_dwordx4 v0,v[20:23],%12 offset:0 sc1\nMM v[72:75],v[216:219],a[120:123],v[72:75]\nv_add_f32 v36,v36,v44\nv_add_f32 v37,v37,v45\n"
"MM v[72:75],v[200:203],a[84:87],v[72:75]\nv_add_f32 v38,v38,v46\nv_add_f32 v39,v39,v47\nMM v[72:75],v[184:187],a[48:51],v[72:75]\n"
"global_store_dwordx4 v0,v[36:39],%12 offset:1024 sc1\nMM v[72:75],v[168:171],a[12:15],v[72:75]\nv_add_f32 v52,v52,v60\nv_add_f32 v53,v53,v61\n"
"MM v[88:91],v[168:171],a[16:19],v[88:91]\nv_add_f32 v54,v54,v62\nv_add_f32 v55,v55,v63\nMM v[88:91],v[184:187],a[52:55],v[88:91]\n"
"global_store_dwordx4 v0,v[52:55],%12 offset:2048 sc1\nMM v[88:91],v[200:203],a[88:91],v[88:91]\nv_add_f32 v68,v68,v76\nv_add_f32 v69,v69,v77\n"
"MM v[88:91],v[216:219],a[124:127],v[88:91]\nv_add_f32 v70,v70,v78\nv_add_f32 v71,v71,v79\nMM v[104:107],v[216:219],a[128:131],v[104:107]\n"
"global_store_dwordx4 v0,v[68:71],%12 offset:3072 sc1\nMM v[104:107],v[200:203],a[92:95],v[104:107]\nv_add_f32 v84,v84,v92\nv_add_f32 v85,v85,v93\n"
"MM v[104:107],v[184:187],a[56:59],v[104:107]\nv_add_f32 v86,v86,v94\nv_add_f32 v87,v87,v95\nMM v[104:107],v[168:171],a[20:23],v[104:107]\n"
"global_store_dwordx4 v3,v[84:87],%12 offset:0 sc1\nMM v[120:123],v[168:171],a[24:27],v[120:123]\nv_add_f32 v100,v100,v108\nv_add_f32 v101,v101,v109\n"
"MM v[120:123],v[184:187],a[60:63],v[120:123]\nv_add_f32 v102,v102,v110\nv_add_f32 v103,v103,v111\nMM v[120:123],v[200:203],a[96:99],v[120:123]\n"
"global_store_dwordx4 v3,v[100:103],%12 offset:1024 sc1\nMM v[120:123],v[216:219],a[132:135],v[120:123]\nv_add_f32 v116,v116,v124\n"
"v_add_f32 v117,v117,v125\nMM v[136:139],v[216:219],a[136:139],v[136:139]\nv_add_f32 v118,v118,v126\nv_add_f32 v119,v119,v127\n"
"MM v[136:139],v[200:203],a[100:103],v[136:139]\nglobal_store_dwordx4 v3,v[116:119],%12 offset:2048 sc1\nMM v[136:139],v[184:187],a[64:67],v[136:139]\n"
"v_add_f32 v132,v132,v140\nv_add_f32 v133,v133,v141\nMM v[136:139],v[168:171],a[28:31],v[136:139]\nv_add_f32 v134,v134,v142\nv_add_f32 v135,v135,v143\n"
"MM v[152:155],v[168:171],a[32:35],v[152:155]\nglobal_store_dwordx4 v3,v[132:135],%12 offset:3072 sc1\nMM v[152:155],v[184:187],a[68:71],v[152:155]\n"
"v_add_f32 v148,v148,v156\nv_add_f32 v149,v149,v157\nMM v[152:155],v[200:203],a[104:107],v[152:155]\nv_add_f32 v150,v150,v158\nv_add_f32 v151,v151,v159\n"
"MM v[152:155],v[216:219],a[140:143],v[152:155]\nglobal_store_dwordx4 v4,v[148:151],%12 offset:0 sc1\nMM v[16:19],v[160:163],a[0:3],v[16:19]\n"
"ds_write_b128 v2,v[24:27] offset:9216\nds_write_b128 v2,v[40:43] offset:10240\nMM v[16:19],v[176:179],a[36:39],v[16:19]\n"
"ds_write_b128 v2,v[56:59] offset:11264\nds_write_b128 v2,v[72:75] offset:12288\nMM v[16:19],v[192:195],a[72:75],v[16:19]\n"
"ds_write_b128 v2,v[88:91] offset:13312\nds_write_b128 v2,v[104:107] offset:14336\nMM v[16:19],v[208:211],a[108:111],v[16:19]\n"
"ds_write_b128 v2,v[120:123] offset:15360\nMM v[32:35],v[208:211],a[112:115],v[32:35]\nds_write_b128 v2,v[136:139] offset:16384\n"
"MM v[32:35],v[192:195],a[76:79],v[32:35]\nMM v[32:35],v[176:179],a[40:43],v[32:35]\nMM v[32:35],v[160:163],a[4:7],v[32:35]\n"
"MM v[48:51],v[160:163],a[8:11],v[48:51]\nds_write_b128 v2,v[152:155] offset:17408\nMM v[48:51],v[176:179],a[44:47],v[48:51]\n"
"MM v[48:51],v[192:195],a[80:83],v[48:51]\nMM v[48:51],v[208:211],a[116:119],v[48:51]\nMM v[64:67],v[208:211],a[120:123],v[64:67]\n"
"MM v[64:67],v[192:195],a[84:87],v[64:67]\nMM v[64:67],v[176:179],a[48:51],v[64:67]\nMM v[64:67],v[160:163],a[12:15],v[64:67]\n"
"MM v[80:83],v[160:163],a[16:19],v[80:83]\ns_waitcnt lgkmcnt(0)\ns_barrier\nMM v[80:83],v[176:179],a[52:55],v[80:83]\n"
"ds_read_b128 v[24:27],v1 offset:9216\nds_read_b128 v[40:43],v1 offset:10240\nMM v[80:83],v[192:195],a[88:91],v[80:83]\n"
"ds_read_b128 v[56:59],v1 offset:11264\nds_read_b128 v[72:75],v1 offset:12288\nMM v[80:83],v[208:211],a[124:127],v[80:83]\n"
"ds_read_b128 v[88:91],v1 offset:13312\nds_read_b128 v[104:107],v1 offset:14336\nMM v[96:99],v[208:211],a[128:131],v[96:99]\n"
"ds_read_b128 v[120:123],v1 offset:15360\nds_read_b128 v[136:139],v1 offset:16384\nMM v[96:99],v[192:195],a[92:95],v[96:99]\n"
"ds_read_b128 v[152:155],v1 offset:17408\nMM v[96:99],v[176:179],a[56:59],v[96:99]\nMM v[96:99],v[160:163],a[20:23],v[96:99]\n"
"MM v[112:115],v[160:163],a[24:27],v[112:115]\nMM v[112:115],v[176:179],a[60:63],v[112:115]\nMM v[112:115],v[192:195],a[96:99],v[112:115]\n"
"MM v[112:115],v[208:211],a[132:135],v[112:115]\nMM v[128:131],v[208:211],a[136:139],v[128:131]\nMM v[128:131],v[192:195],a[100:103],v[128:131]\n"
"MM v[128:131],v[176:179],a[64:67],v[128:131]\nMM v[128:131],v[160:163],a[28:31],v[128:131]\nMM v[144:147],v[160:163],a[32:35],v[144:147]\n"
"MM v[144:147],v[176:179],a[68:71],v[144:147]\nMM v[144:147],v[192:195],a[104:107],v[144:147]\nMM v[144:147],v[208:211],a[140:143],v[144:147]\n"
"s_waitcnt vmcnt(0)\ns_mov_b64 exec,1\nglobal_store_dwordx2 v12,v[10:11],%14 sc1\ns_mov_b64 exec,-1\ns_waitcnt lgkmcnt(0)\n"
"v_pk_add_f32 v[16:17],v[16:17],v[24:25]\nv_pk_add_f32 v[18:19],v[18:19],v[26:27]\nv_pk_add_f32 v[32:33],v[32:33],v[40:41]\n"
"v_pk_add_f32 v[34:35],v[34:35],v[42:43]\nv_pk_add_f32 v[48:49],v[48:49],v[56:57]\nv_pk_add_f32 v[50:51],v[50:51],v[58:59]\n"
"v_pk_add_f32 v[64:65],v[64:65],v[72:73]\nv_pk_add_f32 v[66:67],v[66:67],v[74:75]\nv_pk_add_f32 v[80:81],v[80:81],v[88:89]\n"
"v_pk_add_f32 v[82:83],v[82:83],v[90:91]\nv_pk_add_f32 v[96:97],v[96:97],v[104:105]\nv_pk_add_f32 v[98:99],v[98:99],v[106:107]\n"
"v_pk_add_f32 v[112:113],v[112:113],v[120:121]\nv_pk_add_f32 v[114:115],v[114:115],v[122:123]\nv_pk_add_f32 v[128:129],v[128:129],v[136:137]\n"
"v_pk_add_f32 v[130:131],v[130:131],v[138:139]\nv_pk_add_f32 v[144:145],v[144:145],v[152:153]\nv_pk_add_f32 v[146:147],v[146:147],v[154:155]\n"
"s_mov_b32 s57,0x400000\nL_xpoll_%=:\nglobal_load_dwordx2 v[224:225],v12,%15 sc1\ns_waitcnt vmcnt(0)\nv_cmp_ne_u64 vcc,v[224:225],v[10:11]\ns_nop 4\n"
"s_cbranch_vccz L_xpolle_%=\ns_sleep 1\ns_sub_u32 s57,s57,1\ns_cmp_lg_u32 s57,0\ns_cbranch_scc1 L_xpoll_%=\nL_xpolle_%=:\n"
"global_load_dwordx4 v[20:23],v0,%13 offset:0 sc1\nglobal_load_dwordx4 v[36:39],v0,%13 offset:1024 sc1\n"
"global_load_dwordx4 v[52:55],v0,%13 offset:2048 sc1\nglobal_load_dwordx4 v[68:71],v0,%13 offset:3072 sc1\n"
"global_load_dwordx4 v[84:87],v3,%13 offset:0 sc1\nglobal_load_dwordx4 v[100:103],v3,%13 offset:1024 sc1\n"
"global_load_dwordx4 v[116:119],v3,%13 offset:2048 sc1\nglobal_load_dwordx4 v[132:135],v3,%13 offset:3072 sc1\n"
"global_load_dwordx4 v[148:151],v4,%13 offset:0 sc1\ns_waitcnt vmcnt(8)\nv_pk_add_f32 v[16:17],v[16:17],v[20:21]\n"
"v_pk_add_f32 v[18:19],v[18:19],v[22:23]\nCV v16,v16,v17\nCV v17,v18,v19\nbuffer_store_dwordx2 v[16:17],v7,s[44:47],s58 offen offset:0\n"
"s_add_u32 s58,s58,40960\ns_waitcnt vmcnt(8)\nv_pk_add_f32 v[32:33],v[32:33],v[36:37]\nv_pk_add_f32 v[34:35],v[34:35],v[38:39]\nCV v32,v32,v33\n"
"CV v33,v34,v35\nbuffer_store_dwordx2 v[32:33],v7,s[44:47],s58 offen offset:0\ns_add_u32 s58,s58,40960\ns_waitcnt vmcnt(8)\n"
"v_pk_add_f32 v[48:49],v[48:49],v[52:53]\nv_pk_add_f32 v[50:51],v[50:51],v[54:55]\nCV v48,v48,v49\nCV v49,v50,v51\n"
"buffer_store_dwordx2 v[48:49],v7,s[44:47],s58 offen offset:0\ns_add_u32 s58,s58,40960\ns_waitcnt vmcnt(8)\nv_pk_add_f32 v[64:65],v[64:65],v[68:69]\n"
"v_pk_add_f32 v[66:67],v[66:67],v[70:71]\nCV v64,v64,v65\nCV v65,v66,v67\nbuffer_store_dwordx2 v[64:65],v7,s[44:47],s58 offen offset:0\n"
"s_add_u32 s58,s58,40960\ns_waitcnt vmcnt(8)\nv_pk_add_f32 v[80:81],v[80:81],v[84:85]\nv_pk_add_f32 v[82:83],v[82:83],v[86:87]\nCV v80,v80,v81\n"
"CV v81,v82,v83\nbuffer_store_dwordx2 v[80:81],v7,s[44:47],s58 offen offset:0\ns_add_u32 s58,s58,40960\ns_waitcnt vmcnt(8)\n"
"v_pk_add_f32 v[96:97],v[96:97],v[100:101]\nv_pk_add_f32 v[98:99],v[98:99],v[102:103]\nCV v96,v96,v97\nCV v97,v98,v99\n"
"buffer_store_dwordx2 v[96:97],v7,s[44:47],s58 offen offset:0\ns_add_u32 s58,s58,40960\ns_waitcnt vmcnt(8)\n"
"v_pk_add_f32 v[112:113],v[112:113],v[116:117]\nv_pk_add_f32 v[114:115],v[114:115],v[118:119]\nCV v112,v112,v113\nCV v113,v114,v115\n"
"buffer_store_dwordx2 v[112:113],v7,s[44:47],s58 offen offset:0\ns_add_u32 s58,s58,40960\ns_waitcnt vmcnt(8)\n"
"v_pk_add_f32 v[128:129],v[128:129],v[132:133]\nv_pk_add_f32 v[130:131],v[130:131],v[134:135]\nCV v128,v128,v129\nCV v129,v130,v131\n"
"buffer_store_dwordx2 v[128:129],v7,s[44:47],s58 offen offset:0\ns_add_u32 s58,s58,40960\ns_waitcnt vmcnt(8)\n"
"v_pk_add_f32 v[144:145],v[144:145],v[148:149]\nv_pk_add_f32 v[146:147],v[146:147],v[150:151]\nCV v144,v144,v145\nCV v145,v146,v147\n"
"buffer_store_dwordx2 v[144:145],v7,s[44:47],s58 offen offset:0\n"
".purgem MM\n"
".purgem CV\n"
".purgem TR\n"
      :
      : "s"(Ob + ((kh * 64u + kp) * 104u + rb0 + nh * 5u) * 1024u), "s"((64u * 104u - (kp * 104u + rb0 + nh * 5u)) * 1024u),
        "s"(wop + (kh * 2048u + kp * 32u) * 1280u + ng * 256u + nh * 128u), "s"((2048u - kp * 32u) * 1280u - ng * 256u - nh * 128u),
        "s"(w), "s"((nh && nrb == 8u) ? 0x40000000u : 0u), "s"(nh << 30), "s"(flags + (br * 8u + b) * 16u + j), "s"(flags), "s"(tagu),
        "s"(((br * 8u + rb0 / 13u) * 128u + kh * 64u) | (((br * 8u + (rb0 + nrb - 1u) / 13u) * 128u + kh * 64u) << 16)), "s"(kp + 2u * kh),
        "s"(Of + WS_XC + (bid * 4u + w) * 18432u), "s"(Of + WS_XC + ((bid ^ 2u) * 4u + w) * 18432u), "s"(flags + 512u + (bid * 4u + w) * 16u), "s"(flags + 512u + ((bid ^ 2u) * 4u + w) * 16u),
        "s"(outp + (size_t)rb0 * 16u * 2560u + br * 1280u + ng * 256u + nh * 128u), "s"(nrb * 16u * 2560u - 2560u + 128u)
      : "v0","v1","v2","v3","v4","v5","v6","v7","v8","v9","v10","v11","v12","v13","v14","v15","v16","v17","v18","v19","v20","v21","v22","v23","v24","v25","v26","v27","v28","v29","v30","v31","v32","v33","v34","v35","v36","v37","v38","v39","v40","v41","v42","v43","v44","v45","v46","v47","v48","v49","v50","v51","v52","v53","v54","v55","v56","v57","v58","v59","v60","v61","v62","v63","v64","v65","v66","v67","v68","v69","v70","v71","v72","v73","v74","v75","v76","v77","v78","v79","v80","v81","v82","v83","v84","v85","v86","v87","v88","v89","v90","v91","v92","v93","v94","v95","v96","v97","v98","v99","v100","v101","v102","v103","v104","v105","v106","v107","v108","v109","v110","v111","v112","v113","v114","v115","v116","v117","v118","v119","v120","v121","v122","v123","v124","v125","v126","v127","v128","v129","v130","v131","v132","v133","v134","v135","v136","v137","v138","v139","v140","v141","v142","v143","v144","v145","v146","v147","v148","v149","v150","v151","v152","v153","v154","v155","v156","v157","v158","v159","v160","v161","v162","v163","v164","v165","v166","v167","v168","v169","v170","v171","v172","v173","v174","v175","v176","v177","v178","v179","v180","v181","v182","v183","v184","v185","v186","v187","v188","v189","v190","v191","v192","v193","v194","v195","v196","v197","v198","v199","v200","v201","v202","v203","v204","v205","v206","v207","v208","v209","v210","v211","v212","v213","v214","v215","v216","v217","v218","v219","v220","v221","v222","v223","v224","v225","v226","v227","v228","v229","v230","v231","v232","v233","v234","v235","v236","v237","v238","v239","v240","v241","v242","v243","v244","v245","v246","v247","a0","a1","a2","a3","a4","a5","a6","a7","a8","a9","a10","a11","a12","a13","a14","a15","a16","a17","a18","a19","a20","a21","a22","a23","a24","a25","a26","a27","a28","a29","a30","a31","a32","a33","a34","a35","a36","a37","a38","a39","a40","a41","a42","a43","a44","a45","a46","a47","a48","a49","a50","a51","a52","a53","a54","a55","a56","a57","a58","a59","a60","a61","a62","a63","a64","a65","a66","a67","a68","a69","a70","a71","a72","a73","a74","a75","a76","a77","a78","a79","a80","a81","a82","a83","a84","a85","a86","a87","a88","a89","a90","a91","a92","a93","a94","a95","a96","a97","a98","a99","a100","a101","a102","a103","a104","a105","a106","a107","a108","a109","a110","a111","a112","a113","a114","a115","a116","a117","a118","a119","a120","a121","a122","a123","a124","a125","a126","a127","a128","a129","a130","a131","a132","a133","a134","a135","a136","a137","a138","a139","a140","a141","a142","a143","a144","a145","a146","a147","a148","a149","a150","a151","a152","a153","a154","a155","a156","a157","a158","a159","a160","a161","a162","a163","a164","a165","a166","a167","a168","a169","a170","a171","a172","a173","a174","a175","a176","a177","a178","a179","a180","a181","a182","a183","a184","a185","a186","a187","a188","a189","a190","a191","a192","a193","a194","a195","a196","a197","a198","a199","a200","a201","a202","a203","a204","a205","a206","a207","a208","a209","a210","a211","a212","a213","a214","a215","a216","a217","a218","a219","a220","a221","a222","a223","a224","a225","a226","a227","a228","a229","a230","a231","a232","a233","a234","a235","a236","a237","a238","a239","a240","a241","a242","a243","a244","a245","a246","a247","a248","a249","a250","a251","a252","a253","a254","a255","s40","s41","s42","s43","s44","s45","s46","s47","s48","s49","s50","s51","s52","s53","s54","s55","s56","s57","s58","s59","s60","s61","s62","s63","s64","s65","s66","s67","s68","s69","s70","s71","s72","s73","s74","s75","s76","s77","vcc","scc","memory");
}

extern "C" void kernel_launch(void* const* d_in, const int* in_sizes, int n_in, void* d_out, int out_size, void* d_ws, size_t ws_size, hipStream_t stream) {
  (void)hipFuncSetAttribute((const void*)fused_block, hipFuncAttributeMaxDynamicSharedMemorySize, 163840);
  hipLaunchKernelGGL(fused_block, dim3(256), dim3(256), 163840, stream, (const u16*)d_in[0], (const u16*)d_in[1], (const u16*)d_in[2], (const u16*)d_in[3], (const u16*)d_in[4],
                     (const u16*)d_in[5], (const u16*)d_in[6], (const u16*)d_in[7], (const u16*)d_in[8], (const u16*)d_in[9], (char*)d_ws, (u16*)d_out);
}
```
